```python
import torch

def _softmax(x, axis=-1):
    return torch.softmax(x, dim=axis)
def _gelu(x):
    return torch.nn.functional.gelu(x, approximate="tanh")
def _silu(x):
    return torch.nn.functional.silu(x)
def _sigmoid(x):
    return torch.sigmoid(x)
def _layernorm(x, axis=-1, eps=1e-5):
    m = x.mean(dim=axis, keepdim=True)
    v = x.var(dim=axis, keepdim=True, unbiased=False)
    return (x - m) / torch.sqrt(v + eps)
def _scatter_add(tgt, idx, src, axis=0):
    return tgt.clone().index_add_(axis, idx, src)
def _segment_sum(src, seg, n):
    out = torch.zeros((n,) + src.shape[1:], dtype=src.dtype)
    return out.index_add_(0, seg, src)
def _pad(x, pw):
    flat = tuple(v for pair in reversed(pw) for v in pair)
    return torch.nn.functional.pad(x, flat)

kernel_name = 'parallel_sum__sliding_local_dilated__glu_gated_reglu__prompt'

def setup_inputs():

    _w = (lambda a: (lambda t: t.to(torch.bfloat16) if t.is_floating_point() else t)(torch.as_tensor(a)))
    def _A_setup():
        B, S, D = 1, 2048, 640
        return {
            "x": _w((lambda *s: torch.randn(*s, dtype=torch.bfloat16) * 0.05)(B, S, D)),
            "wqkv": _w((lambda *s: torch.randn(*s, dtype=torch.bfloat16) * 0.05)(D, 3 * D)),
            "wo": _w((lambda *s: torch.randn(*s, dtype=torch.bfloat16) * 0.05)(D, D)),
        }
    _A_in = _A_setup()

    def _B_setup():
        B, S, D, F = 1, 2048, 640, 1728
        return {
            "x": _w((lambda *s: torch.randn(*s, dtype=torch.bfloat16) * 0.05)(B, S, D)),
            "W_gate": _w((lambda *s: torch.randn(*s, dtype=torch.bfloat16) * 0.8)(D, F) * (2.0 / D) ** 0.5),
            "W_up": _w((lambda *s: torch.randn(*s, dtype=torch.bfloat16) * 0.8)(D, F) * (2.0 / D) ** 0.5),
            "W_down": _w((lambda *s: torch.randn(*s, dtype=torch.bfloat16) * 0.8)(F, D) * (1.0 / F) ** 0.5),
        }
    _B_in = _B_setup()

    return {
        "A_x": _A_in["x"],
        "A_wqkv": _A_in["wqkv"],
        "A_wo": _A_in["wo"],
        "B_x": _B_in["x"],
        "B_W_gate": _B_in["W_gate"],
        "B_W_up": _B_in["W_up"],
        "B_W_down": _B_in["W_down"],
    }

def reference(A_x, A_wqkv, A_wo, B_x, B_W_gate, B_W_up, B_W_down):
    def _A_ref(x, wqkv, wo):
        B, S, D = x.shape
        H = 32; Dh = D // H
        W, dil = 8, 2
        qkv = (lambda x, *d: x.permute(*d))((x @ wqkv).reshape(B, S, 3, H, Dh), 2, 0, 3, 1, 4)
        q, k, v = qkv[0], qkv[1], qkv[2]
        scale = Dh ** -0.5
        pos = torch.arange(S)
        off = torch.arange(W) * dil
        idx = pos[:, None] - off[None, :]
        mask = idx >= 0
        idx_c = torch.where(mask, idx, 0).reshape(-1)
        kg = k[:, :, idx_c, :].reshape(B, H, S, W, Dh)
        vg = v[:, :, idx_c, :].reshape(B, H, S, W, Dh)
        logits = torch.einsum("bhsd,bhswd->bhsw", q, kg) * scale
        logits = torch.where(mask[None, None, :, :], logits, -1e9)
        attn = _softmax(logits, axis=-1)
        o = torch.einsum("bhsw,bhswd->bhsd", attn, vg)
        return (lambda x, *d: x.permute(*d))(o, 0, 2, 1, 3).reshape(B, S, D) @ wo

    def _B_ref(x, W_gate, W_up, W_down):
        gate = torch.einsum("bsd,df->bsf", x, W_gate)
        up = torch.einsum("bsd,df->bsf", x, W_up)
        gate = gate * (lambda x, dt: x.to(dt))(gate > 0, gate.dtype)
        h = gate * up
        return torch.einsum("bsf,fd->bsd", h, W_down)

    x = A_x
    xn = _layernorm(x, axis=-1)
    return x + _A_ref(xn, A_wqkv, A_wo) + _B_ref(xn, B_W_gate, B_W_up, B_W_down)
```

```cpp
#include <hip/hip_runtime.h>
#include <hip/hip_bf16.h>
#include <stdint.h>

typedef __attribute__((ext_vector_type(8))) short bf16x8;
typedef __attribute__((ext_vector_type(4))) short bf16x4;
typedef __attribute__((ext_vector_type(4))) float f32x4;
typedef unsigned short u16;
#define LDS_AS __attribute__((address_space(3)))

#define S_ 2048
#define D_ 640
#define F_ 1728
#define NQKV 1920
#define KAH 2368

__device__ __forceinline__ float bf2f(u16 v) { return __uint_as_float(((unsigned)v) << 16); }
__device__ __forceinline__ u16 f2bf(float f) {
  unsigned u = __float_as_uint(f);
  u += 0x7FFF + ((u >> 16) & 1);
  return (u16)(u >> 16);
}

__global__ __launch_bounds__(256) void ln_kernel(const u16* __restrict__ x, u16* __restrict__ xn) {
  int row = blockIdx.x * 4 + (threadIdx.x >> 6);
  int lane = threadIdx.x & 63;
  const u16* xr = x + (size_t)row * D_;
  float v[10];
  float s = 0.f;
#pragma unroll
  for (int i = 0; i < 10; ++i) { v[i] = bf2f(xr[lane + i * 64]); s += v[i]; }
#pragma unroll
  for (int o = 32; o > 0; o >>= 1) s += __shfl_xor(s, o);
  float m = s * (1.0f / D_);
  float q = 0.f;
#pragma unroll
  for (int i = 0; i < 10; ++i) { float d = v[i] - m; q += d * d; }
#pragma unroll
  for (int o = 32; o > 0; o >>= 1) q += __shfl_xor(q, o);
  float rs = rsqrtf(q * (1.0f / D_) + 1e-5f);
#pragma unroll
  for (int i = 0; i < 10; ++i) xn[(size_t)row * D_ + lane + i * 64] = f2bf((v[i] - m) * rs);
}

template <int MODE, int BM, int BN, int WM, int WN, int NSTAGE>
__global__ __launch_bounds__(WM* WN * 64) void gemm_kernel(
    const u16* __restrict__ A, int lda, int nk,
    const u16* __restrict__ W0, const u16* __restrict__ W1, const u16* __restrict__ W2,
    u16* __restrict__ out0, u16* __restrict__ out1, const u16* __restrict__ xres) {
  constexpr int T = WM * WN * 64;
  constexpr int TM = BM / WM / 16, TN = BN / WN / 16;
  constexpr int NB = BN / 16;
  constexpr int A_BYTES = BM * 128, B_BYTES = 64 * BN * 2, ST_BYTES = A_BYTES + B_BYTES;
  constexpr int AU = A_BYTES / 16 / T, BU = B_BYTES / 16 / T;
  static_assert(AU * 16 * T == A_BYTES && BU * 16 * T == B_BYTES, "units");
  extern __shared__ __attribute__((aligned(16))) char smem[];

  const int tid = threadIdx.x, lane = tid & 63, wid = tid >> 6;
  const int wm = wid / WN, wn = wid % WN;
  const int fr = lane & 15, fq = lane >> 4;
  int ct, rt;
  if (MODE == 1) { ct = blockIdx.x % 42; rt = blockIdx.x / 42; }
  else { ct = blockIdx.x % (D_ / BN); rt = blockIdx.x / (D_ / BN); }
  const int row0 = rt * BM;

  const u16* asrc[AU];
  const u16* bsrc[BU];
  int bstride;
#pragma unroll
  for (int i = 0; i < AU; ++i) {
    int u = i * T + tid, r = u >> 3, slot = u & 7;
    asrc[i] = A + (size_t)(row0 + r) * lda + ((slot ^ (r & 7)) << 3);
  }
#pragma unroll
  for (int i = 0; i < BU; ++i) {
    int u = i * T + tid, s = u >> 4, rp = (u & 15) >> 1, half = u & 1;
    int kb = s / NB, nb = s % NB;
    int k = kb * 8 + (rp ^ ((kb & 1) << 2));
    int c = nb * 16 + half * 8;
    if (MODE == 1) {
      if (ct < 15) { bsrc[i] = W0 + (size_t)k * NQKV + ct * 128 + c; bstride = NQKV; }
      else {
        int j = ct - 15;
        const u16* w = ((c >> 5) & 1) ? W2 : W1;
        bsrc[i] = w + (size_t)k * F_ + j * 64 + (c >> 6) * 32 + (c & 31);
        bstride = F_;
      }
    } else {
      bsrc[i] = W0 + (size_t)k * D_ + ct * BN + c;
      bstride = D_;
    }
  }

  auto stage = [&](int kt) {
    char* base = smem + (kt % NSTAGE) * ST_BYTES;
#pragma unroll
    for (int i = 0; i < AU; ++i)
      __builtin_amdgcn_global_load_lds((const unsigned*)(asrc[i] + kt * 64),
                                       (LDS_AS unsigned*)(base + (i * T + wid * 64) * 16), 16, 0, 0);
#pragma unroll
    for (int i = 0; i < BU; ++i) {
      const u16* p;
      if (MODE == 1) p = bsrc[i] + (size_t)kt * 64 * bstride;
      else {
        if (kt < 10) p = bsrc[i] + (size_t)kt * 64 * D_;
        else p = (bsrc[i] - W0 + W1) + (size_t)(kt - 10) * 64 * D_;
      }
      __builtin_amdgcn_global_load_lds((const unsigned*)p,
                                       (LDS_AS unsigned*)(base + A_BYTES + (i * T + wid * 64) * 16), 16, 0, 0);
    }
  };

  f32x4 acc[TM][TN];
#pragma unroll
  for (int m = 0; m < TM; ++m)
#pragma unroll
    for (int n = 0; n < TN; ++n) acc[m][n] = (f32x4){0.f, 0.f, 0.f, 0.f};

#pragma unroll
  for (int s = 0; s < NSTAGE - 1; ++s) stage(s);

  for (int kt = 0; kt < nk; ++kt) {
    if (kt + NSTAGE - 2 < nk) {
      if (NSTAGE == 3) asm volatile("s_waitcnt vmcnt(%0)" ::"i"(AU + BU) : "memory");
      else if (NSTAGE == 4) asm volatile("s_waitcnt vmcnt(%0)" ::"i"(2 * (AU + BU)) : "memory");
      else asm volatile("s_waitcnt vmcnt(0)" ::: "memory");
    } else {
      asm volatile("s_waitcnt vmcnt(0)" ::: "memory");
    }
    __builtin_amdgcn_s_barrier();
    if (kt + NSTAGE - 1 < nk) stage(kt + NSTAGE - 1);
    const char* base = smem + (kt % NSTAGE) * ST_BYTES;
    const char* bbase = base + A_BYTES;
#pragma unroll
    for (int ks = 0; ks < 2; ++ks) {
      bf16x8 af[TM], bfr[TN];
#pragma unroll
      for (int m = 0; m < TM; ++m) {
        int r = wm * (BM / WM) + m * 16 + fr;
        af[m] = *(const bf16x8*)(base + r * 128 + (((ks * 4 + fq) ^ (r & 7)) << 4));
      }
#pragma unroll
      for (int n = 0; n < TN; ++n) {
        int nb = wn * TN + n;
        int kb = ks * 4 + fq;
        int q = (lane & 15) >> 2, p = lane & 3;
        bf16x4 lo, hi;
        {
          int rp = (4 * (0 ^ (fq & 1)) + q);
          lo = __builtin_amdgcn_ds_read_tr16_b64_v4i16((LDS_AS bf16x4*)(bbase + (kb * NB + nb) * 256 + rp * 32 + p * 8));
        }
        {
          int rp = (4 * (1 ^ (fq & 1)) + q);
          hi = __builtin_amdgcn_ds_read_tr16_b64_v4i16((LDS_AS bf16x4*)(bbase + (kb * NB + nb) * 256 + rp * 32 + p * 8));
        }
        bfr[n] = __builtin_shufflevector(lo, hi, 0, 1, 2, 3, 4, 5, 6, 7);
      }
#pragma unroll
      for (int m = 0; m < TM; ++m)
#pragma unroll
        for (int n = 0; n < TN; ++n)
          acc[m][n] = __builtin_amdgcn_mfma_f32_16x16x32_bf16(af[m], bfr[n], acc[m][n], 0, 0, 0);
    }
  }
  __syncthreads();

  constexpr int RS = BN * 2 + 16;
  if (MODE == 1 && ct >= 15) {
#pragma unroll
    for (int m = 0; m < TM; ++m)
#pragma unroll
      for (int n = 0; n < TN / 2; ++n)
#pragma unroll
        for (int j = 0; j < 4; ++j) {
          float g = acc[m][n][j], u = acc[m][n + TN / 2][j];
          float h = (g > 0.f ? g : 0.f) * u;
          int r = wm * (BM / WM) + m * 16 + fq * 4 + j;
          int c = wn * 32 + n * 16 + fr;
          *(u16*)(smem + r * RS + c * 2) = f2bf(h);
        }
    __syncthreads();
    for (int u = tid; u < BM * 8; u += T) {
      int r = u >> 3, c = u & 7;
      *(uint4*)(out1 + (size_t)(row0 + r) * KAH + D_ + (ct - 15) * 64 + c * 8) = *(const uint4*)(smem + r * RS + c * 16);
    }
  } else {
#pragma unroll
    for (int m = 0; m < TM; ++m)
#pragma unroll
      for (int n = 0; n < TN; ++n)
#pragma unroll
        for (int j = 0; j < 4; ++j) {
          int r = wm * (BM / WM) + m * 16 + fq * 4 + j;
          int c = wn * (BN / WN) + n * 16 + fr;
          if (MODE == 1) *(u16*)(smem + r * RS + c * 2) = f2bf(acc[m][n][j]);
          else *(float*)(smem + (r * (BN + 4) + c) * 4) = acc[m][n][j];
        }
    __syncthreads();
    if (MODE == 1) {
      for (int u = tid; u < BM * (BN / 8); u += T) {
        int r = u / (BN / 8), c = u % (BN / 8);
        *(uint4*)(out0 + (size_t)(row0 + r) * NQKV + ct * BN + c * 8) = *(const uint4*)(smem + r * RS + c * 16);
      }
    } else {
      for (int u = tid; u < BM * (BN / 8); u += T) {
        int r = u / (BN / 8), c = u % (BN / 8);
        const float* sp = (const float*)(smem + (r * (BN + 4) + c * 8) * 4);
        size_t go = (size_t)(row0 + r) * D_ + ct * BN + c * 8;
        uint4 xv = *(const uint4*)(xres + go);
        const u16* xp = (const u16*)&xv;
        u16 o[8];
#pragma unroll
        for (int e = 0; e < 8; ++e) o[e] = f2bf(bf2f(xp[e]) + sp[e]);
        *(uint4*)(out0 + go) = *(const uint4*)o;
      }
    }
  }
}

__global__ __launch_bounds__(256) void attn_kernel(const u16* __restrict__ qkv, u16* __restrict__ ah) {
  int idx = blockIdx.x * 256 + threadIdx.x;
  int h = idx & 31, s = idx >> 5;
  float q[20];
  {
    const uint2* p = (const uint2*)(qkv + (size_t)s * NQKV + h * 20);
#pragma unroll
    for (int i = 0; i < 5; ++i) {
      uint2 t = p[i];
      q[i * 4 + 0] = __uint_as_float(t.x << 16); q[i * 4 + 1] = __uint_as_float(t.x & 0xffff0000u);
      q[i * 4 + 2] = __uint_as_float(t.y << 16); q[i * 4 + 3] = __uint_as_float(t.y & 0xffff0000u);
    }
  }
  float lg[8];
  const float scale = 0.22360679774997896f;
#pragma unroll
  for (int w = 0; w < 8; ++w) {
    int ks = s - 2 * w;
    int kc = ks >= 0 ? ks : 0;
    const uint2* p = (const uint2*)(qkv + (size_t)kc * NQKV + D_ + h * 20);
    float a = 0.f;
#pragma unroll
    for (int i = 0; i < 5; ++i) {
      uint2 t = p[i];
      a += q[i * 4 + 0] * __uint_as_float(t.x << 16) + q[i * 4 + 1] * __uint_as_float(t.x & 0xffff0000u) +
           q[i * 4 + 2] * __uint_as_float(t.y << 16) + q[i * 4 + 3] * __uint_as_float(t.y & 0xffff0000u);
    }
    lg[w] = ks >= 0 ? a * scale : -1e9f;
  }
  float mx = lg[0];
#pragma unroll
  for (int w = 1; w < 8; ++w) mx = fmaxf(mx, lg[w]);
  float sum = 0.f;
#pragma unroll
  for (int w = 0; w < 8; ++w) { lg[w] = __expf(lg[w] - mx); sum += lg[w]; }
  float inv = 1.0f / sum;
  float o[20];
#pragma unroll
  for (int i = 0; i < 20; ++i) o[i] = 0.f;
#pragma unroll
  for (int w = 0; w < 8; ++w) {
    int ks = s - 2 * w;
    int kc = ks >= 0 ? ks : 0;
    const uint2* p = (const uint2*)(qkv + (size_t)kc * NQKV + 2 * D_ + h * 20);
    float pw = lg[w] * inv;
#pragma unroll
    for (int i = 0; i < 5; ++i) {
      uint2 t = p[i];
      o[i * 4 + 0] += pw * __uint_as_float(t.x << 16); o[i * 4 + 1] += pw * __uint_as_float(t.x & 0xffff0000u);
      o[i * 4 + 2] += pw * __uint_as_float(t.y << 16); o[i * 4 + 3] += pw * __uint_as_float(t.y & 0xffff0000u);
    }
  }
  uint2* op = (uint2*)(ah + (size_t)s * KAH + h * 20);
#pragma unroll
  for (int i = 0; i < 5; ++i) {
    uint2 t;
    t.x = (unsigned)f2bf(o[i * 4 + 0]) | ((unsigned)f2bf(o[i * 4 + 1]) << 16);
    t.y = (unsigned)f2bf(o[i * 4 + 2]) | ((unsigned)f2bf(o[i * 4 + 3]) << 16);
    op[i] = t;
  }
}

extern "C" void kernel_launch(void* const* d_in, const int* in_sizes, int n_in,
                              void* d_out, int out_size, void* d_ws, size_t ws_size,
                              hipStream_t stream) {
  const u16* x = (const u16*)d_in[0];
  const u16* wqkv = (const u16*)d_in[1];
  const u16* wo = (const u16*)d_in[2];
  const u16* wg = (const u16*)d_in[4];
  const u16* wu = (const u16*)d_in[5];
  const u16* wd = (const u16*)d_in[6];
  u16* out = (u16*)d_out;
  u16* xn = (u16*)d_ws;
  u16* qkv = xn + S_ * D_;
  u16* ah = qkv + S_ * NQKV;

  ln_kernel<<<S_ / 4, 256, 0, stream>>>(x, xn);
  {
    constexpr int BM = 128, BN = 128, NS = 3;
    auto k = gemm_kernel<1, BM, BN, 2, 2, NS>;
    int lds = NS * (BM * 128 + 64 * BN * 2);
    hipFuncSetAttribute((const void*)k, hipFuncAttributeMaxDynamicSharedMemorySize, lds);
    k<<<(S_ / BM) * 42, 256, lds, stream>>>(xn, D_, 10, wqkv, wg, wu, qkv, ah, nullptr);
  }
  attn_kernel<<<S_ * 32 / 256, 256, 0, stream>>>(qkv, ah);
  {
    constexpr int BM = 64, BN = 64, NS = 3;
    auto k = gemm_kernel<2, BM, BN, 2, 2, NS>;
    int lds = NS * (BM * 128 + 64 * BN * 2);
    hipFuncSetAttribute((const void*)k, hipFuncAttributeMaxDynamicSharedMemorySize, lds);
    k<<<(S_ / BM) * (D_ / BN), 256, lds, stream>>>(ah, KAH, 37, wo, wd, nullptr, out, nullptr, x);
  }
}
```

```cpp
#define G1BM 256
#define G1BN 192
#define G1NS 2
#define G1WM 4
#define G1WN 2
#define G2BM 64
#define G2BN 64
#define G2NS 4
#define G2WM 2
#define G2WN 2
#include <hip/hip_runtime.h>
#include <hip/hip_bf16.h>
#include <stdint.h>

typedef __attribute__((ext_vector_type(8))) short bf16x8;
typedef __attribute__((ext_vector_type(4))) short bf16x4;
typedef __attribute__((ext_vector_type(4))) float f32x4;
typedef unsigned short u16;
#define LDS_AS __attribute__((address_space(3)))

#define S_ 2048
#define D_ 640
#define F_ 1728
#define NQKV 1920
#define KAH 2368

__device__ __forceinline__ float bf2f(u16 v) { return __uint_as_float(((unsigned)v) << 16); }
__device__ __forceinline__ u16 f2bf(float f) {
  unsigned u = __float_as_uint(f);
  u += 0x7FFF + ((u >> 16) & 1);
  return (u16)(u >> 16);
}

__device__ __forceinline__ unsigned pk2(float a, float b) {
  typedef __attribute__((ext_vector_type(2))) __bf16 bf2;
  bf2 v; v[0] = (__bf16)a; v[1] = (__bf16)b;
  return __builtin_bit_cast(unsigned, v);
}
__global__ __launch_bounds__(256) void ln_kernel(const u16* __restrict__ x, u16* __restrict__ xn) {
  int row = blockIdx.x * 4 + (threadIdx.x >> 6);
  int lane = threadIdx.x & 63;
  const u16* xr = x + (size_t)row * D_;
  float v[10];
  float s = 0.f;
#pragma unroll
  for (int i = 0; i < 10; ++i) { v[i] = bf2f(xr[lane + i * 64]); s += v[i]; }
#pragma unroll
  for (int o = 32; o > 0; o >>= 1) s += __shfl_xor(s, o);
  float m = s * (1.0f / D_);
  float q = 0.f;
#pragma unroll
  for (int i = 0; i < 10; ++i) { float d = v[i] - m; q += d * d; }
#pragma unroll
  for (int o = 32; o > 0; o >>= 1) q += __shfl_xor(q, o);
  float rs = rsqrtf(q * (1.0f / D_) + 1e-5f);
#pragma unroll
  for (int i = 0; i < 10; ++i) xn[(size_t)row * D_ + lane + i * 64] = f2bf((v[i] - m) * rs);
}

template <int MODE, int BM, int BN, int WM, int WN, int NSTAGE>
__global__ __launch_bounds__(WM* WN * 64) void gemm_kernel(
    const u16* __restrict__ A, int lda, int nk,
    const u16* __restrict__ W0, const u16* __restrict__ W1, const u16* __restrict__ W2,
    u16* __restrict__ out0, u16* __restrict__ out1, const u16* __restrict__ xres) {
  constexpr int T = WM * WN * 64;
  constexpr int TM = BM / WM / 16, TN = BN / WN / 16;
  constexpr int NB = BN / 16;
  constexpr int A_BYTES = BM * 128, B_BYTES = 64 * BN * 2, ST_BYTES = A_BYTES + B_BYTES;
  constexpr int AU = A_BYTES / 16 / T, BU = B_BYTES / 16 / T;
  static_assert(AU * 16 * T == A_BYTES && BU * 16 * T == B_BYTES, "units");
  extern __shared__ __attribute__((aligned(16))) char smem[];

  const int tid = threadIdx.x, lane = tid & 63, wid = tid >> 6;
  const int wm = wid / WN, wn = wid % WN;
  const int fr = lane & 15, fq = lane >> 4;
  int ct, rt;
  constexpr int NQT = NQKV / BN, NCT = NQT + 2 * F_ / BN;
  {
    int t = (blockIdx.x & 7) * (gridDim.x >> 3) + (blockIdx.x >> 3);
    if (MODE == 1) { ct = t / (S_ / BM); rt = t % (S_ / BM); }
    else { ct = t % (D_ / BN); rt = t / (D_ / BN); }
  }
  const int row0 = rt * BM;

  const u16* asrc[AU];
  const u16* bsrc[BU];
  int bstride;
#pragma unroll
  for (int i = 0; i < AU; ++i) {
    int u = i * T + tid, r = u >> 3, slot = u & 7;
    asrc[i] = A + (size_t)(row0 + r) * lda + ((slot ^ (r & 7)) << 3);
  }
#pragma unroll
  for (int i = 0; i < BU; ++i) {
    int u = i * T + tid, s = u >> 4, rp = (u & 15) >> 1, half = u & 1;
    int kb = s / NB, nb = s % NB;
    int k = kb * 8 + (rp ^ ((kb & 1) << 2));
    int c = nb * 16 + half * 8;
    if (MODE == 1) {
      if (ct < NQT) { bsrc[i] = W0 + (size_t)k * NQKV + ct * BN + c; bstride = NQKV; }
      else {
        int j = ct - NQT;
        constexpr int WC = BN / WN, HC = WC / 2;
        int cwn = c / WC, cw = c % WC;
        const u16* w = (cw >= HC) ? W2 : W1;
        bsrc[i] = w + (size_t)k * F_ + j * (BN / 2) + cwn * HC + (cw % HC);
        bstride = F_;
      }
    } else {
      bsrc[i] = W0 + (size_t)k * D_ + ct * BN + c;
      bstride = D_;
    }
  }

  float rmean[AU], rrstd[AU];
  uint4 areg[AU];
  if (MODE == 1) {
    static_assert(MODE != 1 || T == 2 * BM, "stats mapping");
    float2* st = (float2*)(smem + NSTAGE * ST_BYTES);
    const uint4* xp = (const uint4*)(A + (size_t)(row0 + (tid >> 1)) * lda + (tid & 1) * 320);
    float s = 0.f;
#pragma unroll 8
    for (int i = 0; i < 40; ++i) {
      uint4 v = xp[i];
      s += (__uint_as_float(v.x << 16) + __uint_as_float(v.x & 0xffff0000u)) + (__uint_as_float(v.y << 16) + __uint_as_float(v.y & 0xffff0000u)) +
           (__uint_as_float(v.z << 16) + __uint_as_float(v.z & 0xffff0000u)) + (__uint_as_float(v.w << 16) + __uint_as_float(v.w & 0xffff0000u));
    }
    s += __shfl_xor(s, 1);
    const float mu = s * (1.0f / D_);
    float qv = 0.f;
#pragma unroll 8
    for (int i = 0; i < 40; ++i) {
      uint4 v = xp[i];
      unsigned w[4] = {v.x, v.y, v.z, v.w};
#pragma unroll
      for (int e = 0; e < 4; ++e) {
        float a = __uint_as_float(w[e] << 16) - mu, b = __uint_as_float(w[e] & 0xffff0000u) - mu;
        qv += a * a + b * b;
      }
    }
    qv += __shfl_xor(qv, 1);
    if ((tid & 1) == 0) st[tid >> 1] = make_float2(mu, rsqrtf(qv * (1.0f / D_) + 1e-5f));
    __syncthreads();
#pragma unroll
    for (int i = 0; i < AU; ++i) { float2 m2 = st[(i * T + tid) >> 3]; rmean[i] = m2.x; rrstd[i] = m2.y; }
  }
  auto aload = [&](int kt) {
#pragma unroll
    for (int i = 0; i < AU; ++i) areg[i] = *(const uint4*)(asrc[i] + kt * 64);
  };
  auto awrite = [&](int kt) {
    char* base = smem + (kt % NSTAGE) * ST_BYTES;
#pragma unroll
    for (int i = 0; i < AU; ++i) {
      unsigned w[4] = {areg[i].x, areg[i].y, areg[i].z, areg[i].w};
      uint4 o;
      unsigned r4[4];
#pragma unroll
      for (int e = 0; e < 4; ++e)
        r4[e] = pk2((__uint_as_float(w[e] << 16) - rmean[i]) * rrstd[i], (__uint_as_float(w[e] & 0xffff0000u) - rmean[i]) * rrstd[i]);
      o.x = r4[0]; o.y = r4[1]; o.z = r4[2]; o.w = r4[3];
      *(uint4*)(base + (i * T + tid) * 16) = o;
    }
  };
  auto stage = [&](int kt) {
    char* base = smem + (kt % NSTAGE) * ST_BYTES;
    if (MODE != 1) {
#pragma unroll
    for (int i = 0; i < AU; ++i)
      __builtin_amdgcn_global_load_lds((const unsigned*)(asrc[i] + kt * 64),
                                       (LDS_AS unsigned*)(base + (i * T + wid * 64) * 16), 16, 0, 0);
    }
#pragma unroll
    for (int i = 0; i < BU; ++i) {
      const u16* p;
      if (MODE == 1) p = bsrc[i] + (size_t)kt * 64 * bstride;
      else {
        if (kt < 10) p = bsrc[i] + (size_t)kt * 64 * D_;
        else p = (bsrc[i] - W0 + W1) + (size_t)(kt - 10) * 64 * D_;
      }
      __builtin_amdgcn_global_load_lds((const unsigned*)p,
                                       (LDS_AS unsigned*)(base + A_BYTES + (i * T + wid * 64) * 16), 16, 0, 0);
    }
  };

  f32x4 acc[TM][TN];
#pragma unroll
  for (int m = 0; m < TM; ++m)
#pragma unroll
    for (int n = 0; n < TN; ++n) acc[m][n] = (f32x4){0.f, 0.f, 0.f, 0.f};

#pragma unroll
  for (int s = 0; s < NSTAGE - 1; ++s) stage(s);
  if (MODE == 1) { static_assert(MODE != 1 || NSTAGE == 2, "ns"); aload(0); awrite(0); }

  const unsigned lds0 = (unsigned)(uintptr_t)(LDS_AS char*)smem;
  unsigned a_off[2], b_off[2];
  {
    const int q = (lane & 15) >> 2, p = lane & 3;
#pragma unroll
    for (int ks = 0; ks < 2; ++ks)
      a_off[ks] = lds0 + (wm * (BM / WM) + fr) * 128 + (((ks * 4 + fq) ^ (fr & 7)) << 4);
#pragma unroll
    for (int t = 0; t < 2; ++t)
      b_off[t] = lds0 + A_BYTES + (fq * NB + wn * TN) * 256 + (4 * (t ^ (fq & 1)) + q) * 32 + p * 8;
  }
  for (int kt = 0; kt < nk; ++kt) {
    if (kt + NSTAGE - 2 < nk) {
      asm volatile("s_waitcnt vmcnt(%0)" ::"i"((NSTAGE - 2) * (AU + BU)) : "memory");
    } else {
      asm volatile("s_waitcnt vmcnt(0)" ::: "memory");
    }
    if (MODE == 1) asm volatile("s_waitcnt lgkmcnt(0)" ::: "memory");
    __builtin_amdgcn_s_barrier();
    const unsigned sb = (kt % NSTAGE) * ST_BYTES;
    bf16x8 af[2][TM];
    bf16x4 bl[2][TN][2];
#pragma unroll
    for (int ks = 0; ks < 2; ++ks) {
      unsigned aa = a_off[ks] + sb, b0 = b_off[0] + sb, b1 = b_off[1] + sb;
#pragma unroll
      for (int m = 0; m < TM; ++m)
        asm volatile("ds_read_b128 %0, %1 offset:%2" : "=v"(af[ks][m]) : "v"(aa), "i"(m * 2048));
#pragma unroll
      for (int n = 0; n < TN; ++n) {
        asm volatile("ds_read_b64_tr_b16 %0, %1 offset:%2" : "=v"(bl[ks][n][0]) : "v"(b0), "i"(ks * 4 * NB * 256 + n * 256));
        asm volatile("ds_read_b64_tr_b16 %0, %1 offset:%2" : "=v"(bl[ks][n][1]) : "v"(b1), "i"(ks * 4 * NB * 256 + n * 256));
      }
    }
    if (kt + NSTAGE - 1 < nk) { stage(kt + NSTAGE - 1); if (MODE == 1) aload(kt + 1); }
#pragma unroll
    for (int ks = 0; ks < 2; ++ks) {
      if (ks == 0) asm volatile("s_waitcnt lgkmcnt(%0)" ::"i"((TM + 2 * TN) > 15 ? 15 : (TM + 2 * TN)) : "memory");
      else asm volatile("s_waitcnt lgkmcnt(0)" ::: "memory");
      __builtin_amdgcn_sched_barrier(0);
#pragma unroll
      for (int m = 0; m < TM; ++m)
#pragma unroll
        for (int n = 0; n < TN; ++n) {
          bf16x8 bb = __builtin_shufflevector(bl[ks][n][0], bl[ks][n][1], 0, 1, 2, 3, 4, 5, 6, 7);
          acc[m][n] = __builtin_amdgcn_mfma_f32_16x16x32_bf16(bb, af[ks][m], acc[m][n], 0, 0, 0);
        }
      __builtin_amdgcn_sched_barrier(0);
    }
    if (MODE == 1 && kt + 1 < nk) awrite(kt + 1);
  }
  __builtin_amdgcn_s_barrier();
  if (MODE == 1) {
    const bool isg = ct >= NQT;
    constexpr int RS = BN * 2 + 16;
    if (isg) {
#pragma unroll
      for (int m = 0; m < TM; ++m)
#pragma unroll
        for (int n = 0; n < TN / 2; ++n) {
          float hv[4];
#pragma unroll
          for (int j = 0; j < 4; ++j) { float g = acc[m][n][j]; hv[j] = (g > 0.f ? g : 0.f) * acc[m][n + TN / 2][j]; }
          uint2 o; o.x = pk2(hv[0], hv[1]); o.y = pk2(hv[2], hv[3]);
          int r = wm * (BM / WM) + m * 16 + fr;
          int c = wn * (BN / WN / 2) + n * 16 + fq * 4;
          *(uint2*)(smem + r * RS + c * 2) = o;
        }
    } else {
#pragma unroll
      for (int m = 0; m < TM; ++m)
#pragma unroll
        for (int n = 0; n < TN; ++n) {
          uint2 o; o.x = pk2(acc[m][n][0], acc[m][n][1]); o.y = pk2(acc[m][n][2], acc[m][n][3]);
          int r = wm * (BM / WM) + m * 16 + fr;
          int c = wn * (BN / WN) + n * 16 + fq * 4;
          *(uint2*)(smem + r * RS + c * 2) = o;
        }
    }
    __syncthreads();
    if (isg) {
      constexpr int CPR = BN / 16;
#pragma unroll
      for (int i = 0; i < BM * CPR / T; ++i) {
        int u = i * T + tid, r = u / CPR, c = u % CPR;
        *(uint4*)(out1 + (size_t)(row0 + r) * KAH + D_ + (ct - NQT) * (BN / 2) + c * 8) = *(const uint4*)(smem + r * RS + c * 16);
      }
    } else {
      constexpr int CPR = BN / 8;
#pragma unroll
      for (int i = 0; i < BM * CPR / T; ++i) {
        int u = i * T + tid, r = u / CPR, c = u % CPR;
        *(uint4*)(out0 + (size_t)(row0 + r) * NQKV + ct * BN + c * 8) = *(const uint4*)(smem + r * RS + c * 16);
      }
    }
  } else {
    constexpr int RS = BN * 4 + 16;
#pragma unroll
    for (int m = 0; m < TM; ++m)
#pragma unroll
      for (int n = 0; n < TN; ++n) {
        int r = wm * (BM / WM) + m * 16 + fr;
        int c = wn * (BN / WN) + n * 16 + fq * 4;
        *(f32x4*)(smem + r * RS + c * 4) = acc[m][n];
      }
    __syncthreads();
    constexpr int CPR = BN / 8;
#pragma unroll
    for (int i = 0; i < BM * CPR / T; ++i) {
      int u = i * T + tid, r = u / CPR, c = u % CPR;
      const f32x4 s0 = *(const f32x4*)(smem + r * RS + c * 32), s1 = *(const f32x4*)(smem + r * RS + c * 32 + 16);
      size_t go = (size_t)(row0 + r) * D_ + ct * BN + c * 8;
      uint4 xv = *(const uint4*)(xres + go);
      uint4 o;
      o.x = pk2(s0[0] + __uint_as_float(xv.x << 16), s0[1] + __uint_as_float(xv.x & 0xffff0000u));
      o.y = pk2(s0[2] + __uint_as_float(xv.y << 16), s0[3] + __uint_as_float(xv.y & 0xffff0000u));
      o.z = pk2(s1[0] + __uint_as_float(xv.z << 16), s1[1] + __uint_as_float(xv.z & 0xffff0000u));
      o.w = pk2(s1[2] + __uint_as_float(xv.w << 16), s1[3] + __uint_as_float(xv.w & 0xffff0000u));
      *(uint4*)(out0 + go) = o;
    }
  }
}

__global__ __launch_bounds__(256) void attn_kernel(const u16* __restrict__ qkv, u16* __restrict__ ah) {
  int idx = blockIdx.x * 256 + threadIdx.x;
  int h = idx & 31, s = idx >> 5;
  float q[20];
  {
    const uint2* p = (const uint2*)(qkv + (size_t)s * NQKV + h * 20);
#pragma unroll
    for (int i = 0; i < 5; ++i) {
      uint2 t = p[i];
      q[i * 4 + 0] = __uint_as_float(t.x << 16); q[i * 4 + 1] = __uint_as_float(t.x & 0xffff0000u);
      q[i * 4 + 2] = __uint_as_float(t.y << 16); q[i * 4 + 3] = __uint_as_float(t.y & 0xffff0000u);
    }
  }
  float lg[8];
  const float scale = 0.22360679774997896f;
#pragma unroll
  for (int w = 0; w < 8; ++w) {
    int ks = s - 2 * w;
    int kc = ks >= 0 ? ks : 0;
    const uint2* p = (const uint2*)(qkv + (size_t)kc * NQKV + D_ + h * 20);
    float a = 0.f;
#pragma unroll
    for (int i = 0; i < 5; ++i) {
      uint2 t = p[i];
      a += q[i * 4 + 0] * __uint_as_float(t.x << 16) + q[i * 4 + 1] * __uint_as_float(t.x & 0xffff0000u) +
           q[i * 4 + 2] * __uint_as_float(t.y << 16) + q[i * 4 + 3] * __uint_as_float(t.y & 0xffff0000u);
    }
    lg[w] = ks >= 0 ? a * scale : -1e9f;
  }
  float mx = lg[0];
#pragma unroll
  for (int w = 1; w < 8; ++w) mx = fmaxf(mx, lg[w]);
  float sum = 0.f;
#pragma unroll
  for (int w = 0; w < 8; ++w) { lg[w] = __expf(lg[w] - mx); sum += lg[w]; }
  float inv = 1.0f / sum;
  float o[20];
#pragma unroll
  for (int i = 0; i < 20; ++i) o[i] = 0.f;
#pragma unroll
  for (int w = 0; w < 8; ++w) {
    int ks = s - 2 * w;
    int kc = ks >= 0 ? ks : 0;
    const uint2* p = (const uint2*)(qkv + (size_t)kc * NQKV + 2 * D_ + h * 20);
    float pw = lg[w] * inv;
#pragma unroll
    for (int i = 0; i < 5; ++i) {
      uint2 t = p[i];
      o[i * 4 + 0] += pw * __uint_as_float(t.x << 16); o[i * 4 + 1] += pw * __uint_as_float(t.x & 0xffff0000u);
      o[i * 4 + 2] += pw * __uint_as_float(t.y << 16); o[i * 4 + 3] += pw * __uint_as_float(t.y & 0xffff0000u);
    }
  }
  uint2* op = (uint2*)(ah + (size_t)s * KAH + h * 20);
#pragma unroll
  for (int i = 0; i < 5; ++i) {
    uint2 t;
    t.x = (unsigned)f2bf(o[i * 4 + 0]) | ((unsigned)f2bf(o[i * 4 + 1]) << 16);
    t.y = (unsigned)f2bf(o[i * 4 + 2]) | ((unsigned)f2bf(o[i * 4 + 3]) << 16);
    op[i] = t;
  }
}

extern "C" void kernel_launch(void* const* d_in, const int* in_sizes, int n_in,
                              void* d_out, int out_size, void* d_ws, size_t ws_size,
                              hipStream_t stream) {
  const u16* x = (const u16*)d_in[0];
  const u16* wqkv = (const u16*)d_in[1];
  const u16* wo = (const u16*)d_in[2];
  const u16* wg = (const u16*)d_in[4];
  const u16* wu = (const u16*)d_in[5];
  const u16* wd = (const u16*)d_in[6];
  u16* out = (u16*)d_out;
  u16* xn = (u16*)d_ws;
  u16* qkv = xn + S_ * D_;
  u16* ah = qkv + S_ * NQKV;

  {
    constexpr int BM = G1BM, BN = G1BN, NS = G1NS;
    auto k = gemm_kernel<1, BM, BN, G1WM, G1WN, NS>;
    int lds = NS * (BM * 128 + 64 * BN * 2) + BM * 8;
    hipFuncSetAttribute((const void*)k, hipFuncAttributeMaxDynamicSharedMemorySize, lds);
    k<<<(S_ / BM) * (5376 / BN), G1WM * G1WN * 64, lds, stream>>>(x, D_, 10, wqkv, wg, wu, qkv, ah, nullptr);
  }
  attn_kernel<<<S_ * 32 / 256, 256, 0, stream>>>(qkv, ah);
  {
    constexpr int BM = G2BM, BN = G2BN, NS = G2NS;
    auto k = gemm_kernel<2, BM, BN, G2WM, G2WN, NS>;
    int lds = NS * (BM * 128 + 64 * BN * 2);
    hipFuncSetAttribute((const void*)k, hipFuncAttributeMaxDynamicSharedMemorySize, lds);
    k<<<(S_ / BM) * (D_ / BN), G2WM * G2WN * 64, lds, stream>>>(ah, KAH, 37, wo, wd, nullptr, out, nullptr, x);
  }
}
```

```cpp
#define G1BM 256
#define G1BN 192
#define G1NS 2
#define G1WM 4
#define G1WN 2
#define G2BM 64
#define G2BN 64
#define G2NS 4
#define G2WM 2
#define G2WN 2
#include <hip/hip_runtime.h>
#include <hip/hip_bf16.h>
#include <stdint.h>

typedef __attribute__((ext_vector_type(8))) short bf16x8;
typedef __attribute__((ext_vector_type(4))) short bf16x4;
typedef __attribute__((ext_vector_type(4))) float f32x4;
typedef unsigned short u16;
#define LDS_AS __attribute__((address_space(3)))

#define S_ 2048
#define D_ 640
#define F_ 1728
#define NQKV 1920
#define KAH 2368

__device__ __forceinline__ float bf2f(u16 v) { return __uint_as_float(((unsigned)v) << 16); }
__device__ __forceinline__ u16 f2bf(float f) {
  unsigned u = __float_as_uint(f);
  u += 0x7FFF + ((u >> 16) & 1);
  return (u16)(u >> 16);
}

__device__ __forceinline__ unsigned pk2(float a, float b) {
  typedef __attribute__((ext_vector_type(2))) __bf16 bf2;
  bf2 v; v[0] = (__bf16)a; v[1] = (__bf16)b;
  return __builtin_bit_cast(unsigned, v);
}
__global__ __launch_bounds__(256) void ln_kernel(const u16* __restrict__ x, u16* __restrict__ xn) {
  int row = blockIdx.x * 4 + (threadIdx.x >> 6);
  int lane = threadIdx.x & 63;
  const u16* xr = x + (size_t)row * D_;
  float v[10];
  float s = 0.f;
#pragma unroll
  for (int i = 0; i < 10; ++i) { v[i] = bf2f(xr[lane + i * 64]); s += v[i]; }
#pragma unroll
  for (int o = 32; o > 0; o >>= 1) s += __shfl_xor(s, o);
  float m = s * (1.0f / D_);
  float q = 0.f;
#pragma unroll
  for (int i = 0; i < 10; ++i) { float d = v[i] - m; q += d * d; }
#pragma unroll
  for (int o = 32; o > 0; o >>= 1) q += __shfl_xor(q, o);
  float rs = rsqrtf(q * (1.0f / D_) + 1e-5f);
#pragma unroll
  for (int i = 0; i < 10; ++i) xn[(size_t)row * D_ + lane + i * 64] = f2bf((v[i] - m) * rs);
}

template <int MODE, int BM, int BN, int WM, int WN, int NSTAGE>
__global__ __launch_bounds__(WM* WN * 64) void gemm_kernel(
    const u16* __restrict__ A, int lda, int nk,
    const u16* __restrict__ W0, const u16* __restrict__ W1, const u16* __restrict__ W2,
    u16* __restrict__ out0, u16* __restrict__ out1, const u16* __restrict__ xres) {
  constexpr int T = WM * WN * 64;
  constexpr int TM = BM / WM / 16, TN = BN / WN / 16;
  constexpr int NB = BN / 16;
  constexpr int A_BYTES = BM * 128, B_BYTES = 64 * BN * 2, ST_BYTES = A_BYTES + B_BYTES;
  constexpr int AU = A_BYTES / 16 / T, BU = B_BYTES / 16 / T;
  static_assert(AU * 16 * T == A_BYTES && BU * 16 * T == B_BYTES, "units");
  extern __shared__ __attribute__((aligned(16))) char smem[];

  const int tid = threadIdx.x, lane = tid & 63, wid = tid >> 6;
  const int wm = wid / WN, wn = wid % WN;
  const int fr = lane & 15, fq = lane >> 4;
  int ct, rt;
  constexpr int NQT = NQKV / BN, NCT = NQT + 2 * F_ / BN;
  {
    int t = (blockIdx.x & 7) * (gridDim.x >> 3) + (blockIdx.x >> 3);
    if (MODE == 1) { ct = t / (S_ / BM); rt = t % (S_ / BM); }
    else { ct = t % (D_ / BN); rt = t / (D_ / BN); }
  }
  const int row0 = rt * BM;

  const u16* asrc[AU];
  const u16* bsrc[BU];
  int bstride;
#pragma unroll
  for (int i = 0; i < AU; ++i) {
    int u = i * T + tid, r = u >> 3, slot = u & 7;
    asrc[i] = A + (size_t)(row0 + r) * lda + ((slot ^ (r & 7)) << 3);
  }
#pragma unroll
  for (int i = 0; i < BU; ++i) {
    int u = i * T + tid, s = u >> 4, rp = (u & 15) >> 1, half = u & 1;
    int kb = s / NB, nb = s % NB;
    int k = kb * 8 + (rp ^ ((kb & 1) << 2));
    int c = nb * 16 + half * 8;
    if (MODE == 1) {
      if (ct < NQT) { bsrc[i] = W0 + (size_t)k * NQKV + ct * BN + c; bstride = NQKV; }
      else {
        int j = ct - NQT;
        constexpr int WC = BN / WN, HC = WC / 2;
        int cwn = c / WC, cw = c % WC;
        const u16* w = (cw >= HC) ? W2 : W1;
        bsrc[i] = w + (size_t)k * F_ + j * (BN / 2) + cwn * HC + (cw % HC);
        bstride = F_;
      }
    } else {
      bsrc[i] = W0 + (size_t)k * D_ + ct * BN + c;
      bstride = D_;
    }
  }

  auto stage = [&](int kt) {
    char* base = smem + (kt % NSTAGE) * ST_BYTES;
#pragma unroll
    for (int i = 0; i < AU; ++i)
      __builtin_amdgcn_global_load_lds((const unsigned*)(asrc[i] + kt * 64),
                                       (LDS_AS unsigned*)(base + (i * T + wid * 64) * 16), 16, 0, 0);
#pragma unroll
    for (int i = 0; i < BU; ++i) {
      const u16* p;
      if (MODE == 1) p = bsrc[i] + (size_t)kt * 64 * bstride;
      else {
        if (kt < 10) p = bsrc[i] + (size_t)kt * 64 * D_;
        else p = (bsrc[i] - W0 + W1) + (size_t)(kt - 10) * 64 * D_;
      }
      __builtin_amdgcn_global_load_lds((const unsigned*)p,
                                       (LDS_AS unsigned*)(base + A_BYTES + (i * T + wid * 64) * 16), 16, 0, 0);
    }
  };

  f32x4 acc[TM][TN];
#pragma unroll
  for (int m = 0; m < TM; ++m)
#pragma unroll
    for (int n = 0; n < TN; ++n) acc[m][n] = (f32x4){0.f, 0.f, 0.f, 0.f};

#pragma unroll
  for (int s = 0; s < NSTAGE - 1; ++s) stage(s);

  const unsigned lds0 = (unsigned)(uintptr_t)(LDS_AS char*)smem;
  unsigned a_off[2], b_off[2];
  {
    const int q = (lane & 15) >> 2, p = lane & 3;
#pragma unroll
    for (int ks = 0; ks < 2; ++ks)
      a_off[ks] = lds0 + (wm * (BM / WM) + fr) * 128 + (((ks * 4 + fq) ^ (fr & 7)) << 4);
#pragma unroll
    for (int t = 0; t < 2; ++t)
      b_off[t] = lds0 + A_BYTES + (fq * NB + wn * TN) * 256 + (4 * (t ^ (fq & 1)) + q) * 32 + p * 8;
  }
  for (int kt = 0; kt < nk; ++kt) {
    if (kt + NSTAGE - 2 < nk) {
      asm volatile("s_waitcnt vmcnt(%0)" ::"i"((NSTAGE - 2) * (AU + BU)) : "memory");
    } else {
      asm volatile("s_waitcnt vmcnt(0)" ::: "memory");
    }
    __builtin_amdgcn_s_barrier();
    const unsigned sb = (kt % NSTAGE) * ST_BYTES;
    bf16x8 af[2][TM];
    bf16x4 bl[2][TN][2];
#pragma unroll
    for (int ks = 0; ks < 2; ++ks) {
      unsigned aa = a_off[ks] + sb, b0 = b_off[0] + sb, b1 = b_off[1] + sb;
#pragma unroll
      for (int m = 0; m < TM; ++m)
        asm volatile("ds_read_b128 %0, %1 offset:%2" : "=v"(af[ks][m]) : "v"(aa), "i"(m * 2048));
#pragma unroll
      for (int n = 0; n < TN; ++n) {
        asm volatile("ds_read_b64_tr_b16 %0, %1 offset:%2" : "=v"(bl[ks][n][0]) : "v"(b0), "i"(ks * 4 * NB * 256 + n * 256));
        asm volatile("ds_read_b64_tr_b16 %0, %1 offset:%2" : "=v"(bl[ks][n][1]) : "v"(b1), "i"(ks * 4 * NB * 256 + n * 256));
      }
    }
    if (kt + NSTAGE - 1 < nk) stage(kt + NSTAGE - 1);
#pragma unroll
    for (int ks = 0; ks < 2; ++ks) {
      if (ks == 0) asm volatile("s_waitcnt lgkmcnt(%0)" ::"i"((TM + 2 * TN) > 15 ? 15 : (TM + 2 * TN)) : "memory");
      else asm volatile("s_waitcnt lgkmcnt(0)" ::: "memory");
      __builtin_amdgcn_sched_barrier(0);
#pragma unroll
      for (int m = 0; m < TM; ++m)
#pragma unroll
        for (int n = 0; n < TN; ++n) {
          bf16x8 bb = __builtin_shufflevector(bl[ks][n][0], bl[ks][n][1], 0, 1, 2, 3, 4, 5, 6, 7);
          acc[m][n] = __builtin_amdgcn_mfma_f32_16x16x32_bf16(bb, af[ks][m], acc[m][n], 0, 0, 0);
        }
      __builtin_amdgcn_sched_barrier(0);
    }
  }
  __builtin_amdgcn_s_barrier();
  if (MODE == 1) {
    const bool isg = ct >= NQT;
    constexpr int RS = BN * 2 + 16;
    if (isg) {
#pragma unroll
      for (int m = 0; m < TM; ++m)
#pragma unroll
        for (int n = 0; n < TN / 2; ++n) {
          float hv[4];
#pragma unroll
          for (int j = 0; j < 4; ++j) { float g = acc[m][n][j]; hv[j] = (g > 0.f ? g : 0.f) * acc[m][n + TN / 2][j]; }
          uint2 o; o.x = pk2(hv[0], hv[1]); o.y = pk2(hv[2], hv[3]);
          int r = wm * (BM / WM) + m * 16 + fr;
          int c = wn * (BN / WN / 2) + n * 16 + fq * 4;
          *(uint2*)(smem + r * RS + c * 2) = o;
        }
    } else {
#pragma unroll
      for (int m = 0; m < TM; ++m)
#pragma unroll
        for (int n = 0; n < TN; ++n) {
          uint2 o; o.x = pk2(acc[m][n][0], acc[m][n][1]); o.y = pk2(acc[m][n][2], acc[m][n][3]);
          int r = wm * (BM / WM) + m * 16 + fr;
          int c = wn * (BN / WN) + n * 16 + fq * 4;
          *(uint2*)(smem + r * RS + c * 2) = o;
        }
    }
    __syncthreads();
    if (isg) {
      constexpr int CPR = BN / 16;
#pragma unroll
      for (int i = 0; i < BM * CPR / T; ++i) {
        int u = i * T + tid, r = u / CPR, c = u % CPR;
        *(uint4*)(out1 + (size_t)(row0 + r) * KAH + D_ + (ct - NQT) * (BN / 2) + c * 8) = *(const uint4*)(smem + r * RS + c * 16);
      }
    } else {
      constexpr int CPR = BN / 8;
#pragma unroll
      for (int i = 0; i < BM * CPR / T; ++i) {
        int u = i * T + tid, r = u / CPR, c = u % CPR;
        *(uint4*)(out0 + (size_t)(row0 + r) * NQKV + ct * BN + c * 8) = *(const uint4*)(smem + r * RS + c * 16);
      }
    }
  } else {
    constexpr int RS = BN * 4 + 16;
#pragma unroll
    for (int m = 0; m < TM; ++m)
#pragma unroll
      for (int n = 0; n < TN; ++n) {
        int r = wm * (BM / WM) + m * 16 + fr;
        int c = wn * (BN / WN) + n * 16 + fq * 4;
        *(f32x4*)(smem + r * RS + c * 4) = acc[m][n];
      }
    __syncthreads();
    constexpr int CPR = BN / 8;
#pragma unroll
    for (int i = 0; i < BM * CPR / T; ++i) {
      int u = i * T + tid, r = u / CPR, c = u % CPR;
      const f32x4 s0 = *(const f32x4*)(smem + r * RS + c * 32), s1 = *(const f32x4*)(smem + r * RS + c * 32 + 16);
      size_t go = (size_t)(row0 + r) * D_ + ct * BN + c * 8;
      uint4 xv = *(const uint4*)(xres + go);
      uint4 o;
      o.x = pk2(s0[0] + __uint_as_float(xv.x << 16), s0[1] + __uint_as_float(xv.x & 0xffff0000u));
      o.y = pk2(s0[2] + __uint_as_float(xv.y << 16), s0[3] + __uint_as_float(xv.y & 0xffff0000u));
      o.z = pk2(s1[0] + __uint_as_float(xv.z << 16), s1[1] + __uint_as_float(xv.z & 0xffff0000u));
      o.w = pk2(s1[2] + __uint_as_float(xv.w << 16), s1[3] + __uint_as_float(xv.w & 0xffff0000u));
      *(uint4*)(out0 + go) = o;
    }
  }
}

#define G1_ST_BYTES (256 * 64 + 32 * 192 * 2)
#define G1_NS 5
__global__ __launch_bounds__(512) void gemm1_kernel(const u16* __restrict__ X, const u16* __restrict__ Wqkv,
    const u16* __restrict__ Wg, const u16* __restrict__ Wu, u16* __restrict__ qkv, u16* __restrict__ ah) {
  constexpr int BM = 256, BN = 192, T = 512, TM = 4, TN = 6, NB = 12, NQT = 10, NK = 20;
  constexpr int A_BYTES = BM * 64;
  extern __shared__ __attribute__((aligned(16))) char smem[];
  const int tid = threadIdx.x, lane = tid & 63, wid = tid >> 6;
  const int wm = wid >> 1, wn = wid & 1;
  const int fr = lane & 15, fq = lane >> 4;
  const int t = (blockIdx.x & 7) * (gridDim.x >> 3) + (blockIdx.x >> 3);
  const int ct = t >> 3, rt = t & 7;
  const int row0 = rt * BM;
  const bool isg = ct >= NQT;

  const u16* asrc[2];
  const u16* bsrc[2];
  int bstride = isg ? F_ : NQKV;
#pragma unroll
  for (int i = 0; i < 2; ++i) {
    int u = i * T + tid, r = u >> 2, slot = u & 3;
    asrc[i] = X + (size_t)(row0 + r) * D_ + ((slot ^ ((r >> 1) & 3)) << 3);
  }
#pragma unroll
  for (int i = 0; i < 2; ++i) {
    int u = i * T + tid; if (u >= 768) u = tid;
    int s = u >> 4, rp = (u & 15) >> 1, half = u & 1;
    int kb = s / NB, nb = s % NB;
    int k = kb * 8 + (rp ^ ((kb & 1) << 2));
    int c = nb * 16 + half * 8;
    if (!isg) bsrc[i] = Wqkv + (size_t)k * NQKV + ct * BN + c;
    else {
      int cwn = c / 96, cw = c % 96;
      const u16* w = (cw >= 48) ? Wu : Wg;
      bsrc[i] = w + (size_t)k * F_ + (ct - NQT) * 96 + cwn * 48 + (cw % 48);
    }
  }
  auto stage = [&](int kt) {
    char* base = smem + (kt % G1_NS) * G1_ST_BYTES;
#pragma unroll
    for (int i = 0; i < 2; ++i)
      __builtin_amdgcn_global_load_lds((const unsigned*)(asrc[i] + kt * 32), (LDS_AS unsigned*)(base + (i * T + wid * 64) * 16), 16, 0, 0);
    __builtin_amdgcn_global_load_lds((const unsigned*)(bsrc[0] + (size_t)kt * 32 * bstride), (LDS_AS unsigned*)(base + A_BYTES + (wid * 64) * 16), 16, 0, 0);
    if (wid < 4)
      __builtin_amdgcn_global_load_lds((const unsigned*)(bsrc[1] + (size_t)kt * 32 * bstride), (LDS_AS unsigned*)(base + A_BYTES + (T + wid * 64) * 16), 16, 0, 0);
  };
#pragma unroll
  for (int s = 0; s < G1_NS - 1; ++s) stage(s);

  float2* st = (float2*)(smem + G1_NS * G1_ST_BYTES);
  {
    const int sub = lane >> 4, l16 = lane & 15;
#pragma unroll
    for (int hb = 0; hb < 2; ++hb) {
      uint4 v[4][5];
#pragma unroll
      for (int it = 0; it < 4; ++it) {
        const int r = wid * 32 + (hb * 4 + it) * 4 + sub;
        const uint4* xp = (const uint4*)(X + (size_t)(row0 + r) * D_) + l16;
#pragma unroll
        for (int i = 0; i < 5; ++i) v[it][i] = xp[i * 16];
      }
#pragma unroll
      for (int it = 0; it < 4; ++it) {
        const int r = wid * 32 + (hb * 4 + it) * 4 + sub;
        float f[40];
#pragma unroll
        for (int i = 0; i < 5; ++i) {
          unsigned w[4] = {v[it][i].x, v[it][i].y, v[it][i].z, v[it][i].w};
#pragma unroll
          for (int e = 0; e < 4; ++e) { f[i * 8 + e * 2] = __uint_as_float(w[e] << 16); f[i * 8 + e * 2 + 1] = __uint_as_float(w[e] & 0xffff0000u); }
        }
        float s = 0.f;
#pragma unroll
        for (int i = 0; i < 40; ++i) s += f[i];
        s += __shfl_xor(s, 1); s += __shfl_xor(s, 2); s += __shfl_xor(s, 4); s += __shfl_xor(s, 8);
        const float mu = s * (1.0f / D_);
        float qv = 0.f;
#pragma unroll
        for (int i = 0; i < 40; ++i) { float d = f[i] - mu; qv += d * d; }
        qv += __shfl_xor(qv, 1); qv += __shfl_xor(qv, 2); qv += __shfl_xor(qv, 4); qv += __shfl_xor(qv, 8);
        if (l16 == 0) st[r] = make_float2(mu, rsqrtf(qv * (1.0f / D_) + 1e-5f));
      }
    }
  }

  f32x4 acc[TM][TN];
  float csum[TN];
#pragma unroll
  for (int n = 0; n < TN; ++n) { csum[n] = 0.f;
#pragma unroll
    for (int m = 0; m < TM; ++m) acc[m][n] = (f32x4){0.f, 0.f, 0.f, 0.f}; }

  const unsigned lds0 = (unsigned)(uintptr_t)(LDS_AS char*)smem;
  const unsigned a_off = lds0 + (wm * 64 + fr) * 64 + ((fq ^ ((fr >> 1) & 3)) << 4);
  unsigned b_off[2];
  {
    const int q = (lane & 15) >> 2, p = lane & 3;
#pragma unroll
    for (int tt = 0; tt < 2; ++tt) b_off[tt] = lds0 + A_BYTES + (fq * NB + wn * TN) * 256 + (4 * (tt ^ (fq & 1)) + q) * 32 + p * 8;
  }
  for (int kt = 0; kt < NK; ++kt) {
    if (kt + G1_NS - 2 < NK) {
      if (wid < 4) asm volatile("s_waitcnt vmcnt(12)" ::: "memory");
      else asm volatile("s_waitcnt vmcnt(9)" ::: "memory");
    } else asm volatile("s_waitcnt vmcnt(0)" ::: "memory");
    __builtin_amdgcn_s_barrier();
    const unsigned sb = (kt % G1_NS) * G1_ST_BYTES;
    bf16x8 af[TM];
    bf16x4 bl[TN][2];
    const unsigned aa = a_off + sb, b0 = b_off[0] + sb, b1 = b_off[1] + sb;
#pragma unroll
    for (int n = 0; n < TN; ++n) {
      asm volatile("ds_read_b64_tr_b16 %0, %1 offset:%2" : "=v"(bl[n][0]) : "v"(b0), "i"(n * 256));
      asm volatile("ds_read_b64_tr_b16 %0, %1 offset:%2" : "=v"(bl[n][1]) : "v"(b1), "i"(n * 256));
    }
#pragma unroll
    for (int m = 0; m < TM; ++m)
      asm volatile("ds_read_b128 %0, %1 offset:%2" : "=v"(af[m]) : "v"(aa), "i"(m * 1024));
    if (kt + G1_NS - 1 < NK) stage(kt + G1_NS - 1);
    asm volatile("s_waitcnt lgkmcnt(3)" ::: "memory");
    __builtin_amdgcn_sched_barrier(0);
#pragma unroll
    for (int m = 0; m < TM; ++m) {
      if (m == 1) { asm volatile("s_waitcnt lgkmcnt(2)" ::: "memory"); __builtin_amdgcn_sched_barrier(0); }
      if (m == 2) { asm volatile("s_waitcnt lgkmcnt(1)" ::: "memory"); __builtin_amdgcn_sched_barrier(0); }
      if (m == 3) { asm volatile("s_waitcnt lgkmcnt(0)" ::: "memory"); __builtin_amdgcn_sched_barrier(0); }
#pragma unroll
      for (int n = 0; n < TN; ++n) {
        bf16x8 bb = __builtin_shufflevector(bl[n][0], bl[n][1], 0, 1, 2, 3, 4, 5, 6, 7);
        acc[m][n] = __builtin_amdgcn_mfma_f32_16x16x32_bf16(bb, af[m], acc[m][n], 0, 0, 0);
        if (m == 0) {
          const uint2 u0 = __builtin_bit_cast(uint2, bl[n][0]), u1 = __builtin_bit_cast(uint2, bl[n][1]);
          csum[n] += ((__uint_as_float(u0.x << 16) + __uint_as_float(u0.x & 0xffff0000u)) + (__uint_as_float(u0.y << 16) + __uint_as_float(u0.y & 0xffff0000u))) +
                     ((__uint_as_float(u1.x << 16) + __uint_as_float(u1.x & 0xffff0000u)) + (__uint_as_float(u1.y << 16) + __uint_as_float(u1.y & 0xffff0000u)));
        }
      }
    }
    __builtin_amdgcn_sched_barrier(0);
  }
  float cs[TN][4];
#pragma unroll
  for (int n = 0; n < TN; ++n) {
    float c = csum[n];
    c += __shfl_xor(c, 16); c += __shfl_xor(c, 32);
#pragma unroll
    for (int j = 0; j < 4; ++j) cs[n][j] = __shfl(c, fq * 4 + j);
  }
  __builtin_amdgcn_s_barrier();
  constexpr int RS = BN * 2 + 16;
#pragma unroll
  for (int m = 0; m < TM; ++m) {
    const int r = wm * 64 + m * 16 + fr;
    const float2 ms = st[r];
    if (isg) {
#pragma unroll
      for (int n = 0; n < TN / 2; ++n) {
        float hv[4];
#pragma unroll
        for (int j = 0; j < 4; ++j) {
          float g = (acc[m][n][j] - ms.x * cs[n][j]) * ms.y, u = (acc[m][n + 3][j] - ms.x * cs[n + 3][j]) * ms.y;
          hv[j] = (g > 0.f ? g : 0.f) * u;
        }
        uint2 o; o.x = pk2(hv[0], hv[1]); o.y = pk2(hv[2], hv[3]);
        *(uint2*)(smem + r * RS + (wn * 48 + n * 16 + fq * 4) * 2) = o;
      }
    } else {
#pragma unroll
      for (int n = 0; n < TN; ++n) {
        float v[4];
#pragma unroll
        for (int j = 0; j < 4; ++j) v[j] = (acc[m][n][j] - ms.x * cs[n][j]) * ms.y;
        uint2 o; o.x = pk2(v[0], v[1]); o.y = pk2(v[2], v[3]);
        *(uint2*)(smem + r * RS + (wn * 96 + n * 16 + fq * 4) * 2) = o;
      }
    }
  }
  __syncthreads();
  if (isg) {
#pragma unroll
    for (int i = 0; i < 6; ++i) {
      int u = i * T + tid, r = u / 12, c = u % 12;
      *(uint4*)(ah + (size_t)(row0 + r) * KAH + D_ + (ct - NQT) * 96 + c * 8) = *(const uint4*)(smem + r * RS + c * 16);
    }
  } else {
#pragma unroll
    for (int i = 0; i < 12; ++i) {
      int u = i * T + tid, r = u / 24, c = u % 24;
      *(uint4*)(qkv + (size_t)(row0 + r) * NQKV + ct * BN + c * 8) = *(const uint4*)(smem + r * RS + c * 16);
    }
  }
}

__global__ __launch_bounds__(256) void attn_kernel(const u16* __restrict__ qkv, u16* __restrict__ ah) {
  int idx = blockIdx.x * 256 + threadIdx.x;
  int h = idx & 31, s = idx >> 5;
  float q[20];
  {
    const uint2* p = (const uint2*)(qkv + (size_t)s * NQKV + h * 20);
#pragma unroll
    for (int i = 0; i < 5; ++i) {
      uint2 t = p[i];
      q[i * 4 + 0] = __uint_as_float(t.x << 16); q[i * 4 + 1] = __uint_as_float(t.x & 0xffff0000u);
      q[i * 4 + 2] = __uint_as_float(t.y << 16); q[i * 4 + 3] = __uint_as_float(t.y & 0xffff0000u);
    }
  }
  float lg[8];
  const float scale = 0.22360679774997896f;
#pragma unroll
  for (int w = 0; w < 8; ++w) {
    int ks = s - 2 * w;
    int kc = ks >= 0 ? ks : 0;
    const uint2* p = (const uint2*)(qkv + (size_t)kc * NQKV + D_ + h * 20);
    float a = 0.f;
#pragma unroll
    for (int i = 0; i < 5; ++i) {
      uint2 t = p[i];
      a += q[i * 4 + 0] * __uint_as_float(t.x << 16) + q[i * 4 + 1] * __uint_as_float(t.x & 0xffff0000u) +
           q[i * 4 + 2] * __uint_as_float(t.y << 16) + q[i * 4 + 3] * __uint_as_float(t.y & 0xffff0000u);
    }
    lg[w] = ks >= 0 ? a * scale : -1e9f;
  }
  float mx = lg[0];
#pragma unroll
  for (int w = 1; w < 8; ++w) mx = fmaxf(mx, lg[w]);
  float sum = 0.f;
#pragma unroll
  for (int w = 0; w < 8; ++w) { lg[w] = __expf(lg[w] - mx); sum += lg[w]; }
  float inv = 1.0f / sum;
  float o[20];
#pragma unroll
  for (int i = 0; i < 20; ++i) o[i] = 0.f;
#pragma unroll
  for (int w = 0; w < 8; ++w) {
    int ks = s - 2 * w;
    int kc = ks >= 0 ? ks : 0;
    const uint2* p = (const uint2*)(qkv + (size_t)kc * NQKV + 2 * D_ + h * 20);
    float pw = lg[w] * inv;
#pragma unroll
    for (int i = 0; i < 5; ++i) {
      uint2 t = p[i];
      o[i * 4 + 0] += pw * __uint_as_float(t.x << 16); o[i * 4 + 1] += pw * __uint_as_float(t.x & 0xffff0000u);
      o[i * 4 + 2] += pw * __uint_as_float(t.y << 16); o[i * 4 + 3] += pw * __uint_as_float(t.y & 0xffff0000u);
    }
  }
  uint2* op = (uint2*)(ah + (size_t)s * KAH + h * 20);
#pragma unroll
  for (int i = 0; i < 5; ++i) {
    uint2 t;
    t.x = (unsigned)f2bf(o[i * 4 + 0]) | ((unsigned)f2bf(o[i * 4 + 1]) << 16);
    t.y = (unsigned)f2bf(o[i * 4 + 2]) | ((unsigned)f2bf(o[i * 4 + 3]) << 16);
    op[i] = t;
  }
}

extern "C" void kernel_launch(void* const* d_in, const int* in_sizes, int n_in,
                              void* d_out, int out_size, void* d_ws, size_t ws_size,
                              hipStream_t stream) {
  const u16* x = (const u16*)d_in[0];
  const u16* wqkv = (const u16*)d_in[1];
  const u16* wo = (const u16*)d_in[2];
  const u16* wg = (const u16*)d_in[4];
  const u16* wu = (const u16*)d_in[5];
  const u16* wd = (const u16*)d_in[6];
  u16* out = (u16*)d_out;
  u16* xn = (u16*)d_ws;
  u16* qkv = xn + S_ * D_;
  u16* ah = qkv + S_ * NQKV;

  {
    int lds = G1_NS * G1_ST_BYTES + 256 * 8;
    hipFuncSetAttribute((const void*)gemm1_kernel, hipFuncAttributeMaxDynamicSharedMemorySize, lds);
    gemm1_kernel<<<224, 512, lds, stream>>>(x, wqkv, wg, wu, qkv, ah);
  }
  attn_kernel<<<S_ * 32 / 256, 256, 0, stream>>>(qkv, ah);
  {
    constexpr int BM = G2BM, BN = G2BN, NS = G2NS;
    auto k = gemm_kernel<2, BM, BN, G2WM, G2WN, NS>;
    int lds = NS * (BM * 128 + 64 * BN * 2);
    hipFuncSetAttribute((const void*)k, hipFuncAttributeMaxDynamicSharedMemorySize, lds);
    k<<<(S_ / BM) * (D_ / BN), G2WM * G2WN * 64, lds, stream>>>(ah, KAH, 37, wo, wd, nullptr, out, nullptr, x);
  }
}
```

```cpp
#define G1BM 256
#define G1BN 192
#define G1NS 2
#define G1WM 4
#define G1WN 2
#define G2BM 64
#define G2BN 64
#define G2NS 4
#define G2WM 2
#define G2WN 2
#include <hip/hip_runtime.h>
#include <hip/hip_bf16.h>
#include <stdint.h>

typedef __attribute__((ext_vector_type(8))) short bf16x8;
typedef __attribute__((ext_vector_type(4))) short bf16x4;
typedef __attribute__((ext_vector_type(4))) float f32x4;
typedef unsigned short u16;
#define LDS_AS __attribute__((address_space(3)))

#define S_ 2048
#define D_ 640
#define F_ 1728
#define NQKV 1920
#define KAH 2368

__device__ __forceinline__ float bf2f(u16 v) { return __uint_as_float(((unsigned)v) << 16); }
__device__ __forceinline__ u16 f2bf(float f) {
  unsigned u = __float_as_uint(f);
  u += 0x7FFF + ((u >> 16) & 1);
  return (u16)(u >> 16);
}

__device__ __forceinline__ unsigned pk2(float a, float b) {
  typedef __attribute__((ext_vector_type(2))) __bf16 bf2;
  bf2 v; v[0] = (__bf16)a; v[1] = (__bf16)b;
  return __builtin_bit_cast(unsigned, v);
}
__global__ __launch_bounds__(256) void ln_kernel(const u16* __restrict__ x, u16* __restrict__ xn, unsigned* cnt) {
  if (blockIdx.x == 0 && threadIdx.x < 64) cnt[threadIdx.x] = 0u;
  int row = blockIdx.x * 4 + (threadIdx.x >> 6);
  int lane = threadIdx.x & 63;
  const u16* xr = x + (size_t)row * D_;
  float v[10];
  float s = 0.f;
#pragma unroll
  for (int i = 0; i < 10; ++i) { v[i] = bf2f(xr[lane + i * 64]); s += v[i]; }
#pragma unroll
  for (int o = 32; o > 0; o >>= 1) s += __shfl_xor(s, o);
  float m = s * (1.0f / D_);
  float q = 0.f;
#pragma unroll
  for (int i = 0; i < 10; ++i) { float d = v[i] - m; q += d * d; }
#pragma unroll
  for (int o = 32; o > 0; o >>= 1) q += __shfl_xor(q, o);
  float rs = rsqrtf(q * (1.0f / D_) + 1e-5f);
#pragma unroll
  for (int i = 0; i < 10; ++i) xn[(size_t)row * D_ + lane + i * 64] = f2bf((v[i] - m) * rs);
}

template <int MODE, int BM, int BN, int WM, int WN, int NSTAGE>
__global__ __launch_bounds__(WM* WN * 64) void gemm_kernel(
    const u16* __restrict__ A, int lda, int nk,
    const u16* __restrict__ W0, const u16* __restrict__ W1, const u16* __restrict__ W2,
    u16* __restrict__ out0, u16* __restrict__ out1, const u16* __restrict__ xres) {
  constexpr int T = WM * WN * 64;
  constexpr int TM = BM / WM / 16, TN = BN / WN / 16;
  constexpr int NB = BN / 16;
  constexpr int A_BYTES = BM * 128, B_BYTES = 64 * BN * 2, ST_BYTES = A_BYTES + B_BYTES;
  constexpr int AU = A_BYTES / 16 / T, BU = B_BYTES / 16 / T;
  static_assert(AU * 16 * T == A_BYTES && BU * 16 * T == B_BYTES, "units");
  extern __shared__ __attribute__((aligned(16))) char smem[];

  const int tid = threadIdx.x, lane = tid & 63, wid = tid >> 6;
  const int wm = wid / WN, wn = wid % WN;
  const int fr = lane & 15, fq = lane >> 4;
  int ct, rt;
  constexpr int NQT = NQKV / BN, NCT = NQT + 2 * F_ / BN;
  {
    int t = (blockIdx.x & 7) * (gridDim.x >> 3) + (blockIdx.x >> 3);
    if (MODE == 1) { ct = t / (S_ / BM); rt = t % (S_ / BM); }
    else { ct = t % (D_ / BN); rt = t / (D_ / BN); }
  }
  const int row0 = rt * BM;

  const u16* asrc[AU];
  const u16* bsrc[BU];
  int bstride;
#pragma unroll
  for (int i = 0; i < AU; ++i) {
    int u = i * T + tid, r = u >> 3, slot = u & 7;
    asrc[i] = A + (size_t)(row0 + r) * lda + ((slot ^ (r & 7)) << 3);
  }
#pragma unroll
  for (int i = 0; i < BU; ++i) {
    int u = i * T + tid, s = u >> 4, rp = (u & 15) >> 1, half = u & 1;
    int kb = s / NB, nb = s % NB;
    int k = kb * 8 + (rp ^ ((kb & 1) << 2));
    int c = nb * 16 + half * 8;
    if (MODE == 1) {
      if (ct < NQT) { bsrc[i] = W0 + (size_t)k * NQKV + ct * BN + c; bstride = NQKV; }
      else {
        int j = ct - NQT;
        constexpr int WC = BN / WN, HC = WC / 2;
        int cwn = c / WC, cw = c % WC;
        const u16* w = (cw >= HC) ? W2 : W1;
        bsrc[i] = w + (size_t)k * F_ + j * (BN / 2) + cwn * HC + (cw % HC);
        bstride = F_;
      }
    } else {
      bsrc[i] = W0 + (size_t)k * D_ + ct * BN + c;
      bstride = D_;
    }
  }

  auto stage = [&](int kt) {
    char* base = smem + (kt % NSTAGE) * ST_BYTES;
#pragma unroll
    for (int i = 0; i < AU; ++i)
      __builtin_amdgcn_global_load_lds((const unsigned*)(asrc[i] + kt * 64),
                                       (LDS_AS unsigned*)(base + (i * T + wid * 64) * 16), 16, 0, 0);
#pragma unroll
    for (int i = 0; i < BU; ++i) {
      const u16* p;
      if (MODE == 1) p = bsrc[i] + (size_t)kt * 64 * bstride;
      else {
        if (kt < 10) p = bsrc[i] + (size_t)kt * 64 * D_;
        else p = (bsrc[i] - W0 + W1) + (size_t)(kt - 10) * 64 * D_;
      }
      __builtin_amdgcn_global_load_lds((const unsigned*)p,
                                       (LDS_AS unsigned*)(base + A_BYTES + (i * T + wid * 64) * 16), 16, 0, 0);
    }
  };

  f32x4 acc[TM][TN];
#pragma unroll
  for (int m = 0; m < TM; ++m)
#pragma unroll
    for (int n = 0; n < TN; ++n) acc[m][n] = (f32x4){0.f, 0.f, 0.f, 0.f};

#pragma unroll
  for (int s = 0; s < NSTAGE - 1; ++s) stage(s);

  const unsigned lds0 = (unsigned)(uintptr_t)(LDS_AS char*)smem;
  unsigned a_off[2], b_off[2];
  {
    const int q = (lane & 15) >> 2, p = lane & 3;
#pragma unroll
    for (int ks = 0; ks < 2; ++ks)
      a_off[ks] = lds0 + (wm * (BM / WM) + fr) * 128 + (((ks * 4 + fq) ^ (fr & 7)) << 4);
#pragma unroll
    for (int t = 0; t < 2; ++t)
      b_off[t] = lds0 + A_BYTES + (fq * NB + wn * TN) * 256 + (4 * (t ^ (fq & 1)) + q) * 32 + p * 8;
  }
  for (int kt = 0; kt < nk; ++kt) {
    if (kt + NSTAGE - 2 < nk) {
      asm volatile("s_waitcnt vmcnt(%0)" ::"i"((NSTAGE - 2) * (AU + BU)) : "memory");
    } else {
      asm volatile("s_waitcnt vmcnt(0)" ::: "memory");
    }
    __builtin_amdgcn_s_barrier();
    const unsigned sb = (kt % NSTAGE) * ST_BYTES;
    bf16x8 af[2][TM];
    bf16x4 bl[2][TN][2];
#pragma unroll
    for (int ks = 0; ks < 2; ++ks) {
      unsigned aa = a_off[ks] + sb, b0 = b_off[0] + sb, b1 = b_off[1] + sb;
#pragma unroll
      for (int m = 0; m < TM; ++m)
        asm volatile("ds_read_b128 %0, %1 offset:%2" : "=v"(af[ks][m]) : "v"(aa), "i"(m * 2048));
#pragma unroll
      for (int n = 0; n < TN; ++n) {
        asm volatile("ds_read_b64_tr_b16 %0, %1 offset:%2" : "=v"(bl[ks][n][0]) : "v"(b0), "i"(ks * 4 * NB * 256 + n * 256));
        asm volatile("ds_read_b64_tr_b16 %0, %1 offset:%2" : "=v"(bl[ks][n][1]) : "v"(b1), "i"(ks * 4 * NB * 256 + n * 256));
      }
    }
    if (kt + NSTAGE - 1 < nk) stage(kt + NSTAGE - 1);
#pragma unroll
    for (int ks = 0; ks < 2; ++ks) {
      if (ks == 0) asm volatile("s_waitcnt lgkmcnt(%0)" ::"i"((TM + 2 * TN) > 15 ? 15 : (TM + 2 * TN)) : "memory");
      else asm volatile("s_waitcnt lgkmcnt(0)" ::: "memory");
      __builtin_amdgcn_sched_barrier(0);
#pragma unroll
      for (int m = 0; m < TM; ++m)
#pragma unroll
        for (int n = 0; n < TN; ++n) {
          bf16x8 bb = __builtin_shufflevector(bl[ks][n][0], bl[ks][n][1], 0, 1, 2, 3, 4, 5, 6, 7);
          acc[m][n] = __builtin_amdgcn_mfma_f32_16x16x32_bf16(bb, af[ks][m], acc[m][n], 0, 0, 0);
        }
      __builtin_amdgcn_sched_barrier(0);
    }
  }
  __builtin_amdgcn_s_barrier();
  if (MODE == 1) {
    const bool isg = ct >= NQT;
    constexpr int RS = BN * 2 + 16;
    if (isg) {
#pragma unroll
      for (int m = 0; m < TM; ++m)
#pragma unroll
        for (int n = 0; n < TN / 2; ++n) {
          float hv[4];
#pragma unroll
          for (int j = 0; j < 4; ++j) { float g = acc[m][n][j]; hv[j] = (g > 0.f ? g : 0.f) * acc[m][n + TN / 2][j]; }
          uint2 o; o.x = pk2(hv[0], hv[1]); o.y = pk2(hv[2], hv[3]);
          int r = wm * (BM / WM) + m * 16 + fr;
          int c = wn * (BN / WN / 2) + n * 16 + fq * 4;
          *(uint2*)(smem + r * RS + c * 2) = o;
        }
    } else {
#pragma unroll
      for (int m = 0; m < TM; ++m)
#pragma unroll
        for (int n = 0; n < TN; ++n) {
          uint2 o; o.x = pk2(acc[m][n][0], acc[m][n][1]); o.y = pk2(acc[m][n][2], acc[m][n][3]);
          int r = wm * (BM / WM) + m * 16 + fr;
          int c = wn * (BN / WN) + n * 16 + fq * 4;
          *(uint2*)(smem + r * RS + c * 2) = o;
        }
    }
    __syncthreads();
    if (isg) {
      constexpr int CPR = BN / 16;
#pragma unroll
      for (int i = 0; i < BM * CPR / T; ++i) {
        int u = i * T + tid, r = u / CPR, c = u % CPR;
        *(uint4*)(out1 + (size_t)(row0 + r) * KAH + D_ + (ct - NQT) * (BN / 2) + c * 8) = *(const uint4*)(smem + r * RS + c * 16);
      }
    } else {
      constexpr int CPR = BN / 8;
#pragma unroll
      for (int i = 0; i < BM * CPR / T; ++i) {
        int u = i * T + tid, r = u / CPR, c = u % CPR;
        *(uint4*)(out0 + (size_t)(row0 + r) * NQKV + ct * BN + c * 8) = *(const uint4*)(smem + r * RS + c * 16);
      }
    }
  } else {
    constexpr int RS = BN * 4 + 16;
#pragma unroll
    for (int m = 0; m < TM; ++m)
#pragma unroll
      for (int n = 0; n < TN; ++n) {
        int r = wm * (BM / WM) + m * 16 + fr;
        int c = wn * (BN / WN) + n * 16 + fq * 4;
        *(f32x4*)(smem + r * RS + c * 4) = acc[m][n];
      }
    __syncthreads();
    constexpr int CPR = BN / 8;
#pragma unroll
    for (int i = 0; i < BM * CPR / T; ++i) {
      int u = i * T + tid, r = u / CPR, c = u % CPR;
      const f32x4 s0 = *(const f32x4*)(smem + r * RS + c * 32), s1 = *(const f32x4*)(smem + r * RS + c * 32 + 16);
      size_t go = (size_t)(row0 + r) * D_ + ct * BN + c * 8;
      uint4 xv = *(const uint4*)(xres + go);
      uint4 o;
      o.x = pk2(s0[0] + __uint_as_float(xv.x << 16), s0[1] + __uint_as_float(xv.x & 0xffff0000u));
      o.y = pk2(s0[2] + __uint_as_float(xv.y << 16), s0[3] + __uint_as_float(xv.y & 0xffff0000u));
      o.z = pk2(s1[0] + __uint_as_float(xv.z << 16), s1[1] + __uint_as_float(xv.z & 0xffff0000u));
      o.w = pk2(s1[2] + __uint_as_float(xv.w << 16), s1[3] + __uint_as_float(xv.w & 0xffff0000u));
      *(uint4*)(out0 + go) = o;
    }
  }
}

#define G1_ST_BYTES (256 * 64 + 32 * 192 * 2)
#define G1_NS 5
__global__ __launch_bounds__(512) void gemm1_kernel(const u16* __restrict__ X, const u16* __restrict__ Wqkv,
    const u16* __restrict__ Wg, const u16* __restrict__ Wu, u16* __restrict__ qkv, u16* __restrict__ ah) {
  constexpr int BM = 256, BN = 192, T = 512, TM = 4, TN = 6, NB = 12, NQT = 10, NK = 20;
  constexpr int A_BYTES = BM * 64;
  extern __shared__ __attribute__((aligned(16))) char smem[];
  const int tid = threadIdx.x, lane = tid & 63, wid = tid >> 6;
  const int wm = wid >> 1, wn = wid & 1;
  const int fr = lane & 15, fq = lane >> 4;
  const int t = (blockIdx.x & 7) * (gridDim.x >> 3) + (blockIdx.x >> 3);
  const int ct = t >> 3, rt = t & 7;
  const int row0 = rt * BM;
  const bool isg = ct >= NQT;

  const u16* asrc[2];
  const u16* bsrc[2];
  int bstride = isg ? F_ : NQKV;
#pragma unroll
  for (int i = 0; i < 2; ++i) {
    int u = i * T + tid, r = u >> 2, slot = u & 3;
    asrc[i] = X + (size_t)(row0 + r) * D_ + ((slot ^ ((r >> 1) & 3)) << 3);
  }
#pragma unroll
  for (int i = 0; i < 2; ++i) {
    int u = i * T + tid; if (u >= 768) u = tid;
    int seg = u >> 8, k = (u >> 3) & 31, slot = u & 7;
    int c = seg * 64 + ((slot ^ ((((k >> 1) & 1) | (((k >> 3) & 1) << 1)) << 1)) << 3);
    if (!isg) bsrc[i] = Wqkv + (size_t)k * NQKV + ct * BN + c;
    else {
      int cwn = c / 96, cw = c % 96;
      const u16* w = (cw >= 48) ? Wu : Wg;
      bsrc[i] = w + (size_t)k * F_ + (ct - NQT) * 96 + cwn * 48 + (cw % 48);
    }
  }
  auto stage = [&](int kt) {
    char* base = smem + (kt % G1_NS) * G1_ST_BYTES;
#pragma unroll
    for (int i = 0; i < 2; ++i)
      __builtin_amdgcn_global_load_lds((const unsigned*)(asrc[i] + kt * 32), (LDS_AS unsigned*)(base + (i * T + wid * 64) * 16), 16, 0, 0);
    __builtin_amdgcn_global_load_lds((const unsigned*)(bsrc[0] + (size_t)kt * 32 * bstride), (LDS_AS unsigned*)(base + A_BYTES + (wid * 64) * 16), 16, 0, 0);
    if (wid < 4)
      __builtin_amdgcn_global_load_lds((const unsigned*)(bsrc[1] + (size_t)kt * 32 * bstride), (LDS_AS unsigned*)(base + A_BYTES + (T + wid * 64) * 16), 16, 0, 0);
  };
#pragma unroll
  for (int s = 0; s < G1_NS - 1; ++s) stage(s);

  f32x4 acc[TM][TN];
#pragma unroll
  for (int n = 0; n < TN; ++n) {
#pragma unroll
    for (int m = 0; m < TM; ++m) acc[m][n] = (f32x4){0.f, 0.f, 0.f, 0.f}; }

  const unsigned lds0 = (unsigned)(uintptr_t)(LDS_AS char*)smem;
  const unsigned a_off = lds0 + (wm * 64 + fr) * 64 + ((fq ^ ((fr >> 1) & 3)) << 4);
  unsigned b_offn[2][TN];
  {
    const int q = (lane & 15) >> 2, p = lane & 3;
#pragma unroll
    for (int tt = 0; tt < 2; ++tt) {
      const int k = 8 * fq + 4 * tt + q;
      const int f = (((k >> 1) & 1) | (((k >> 3) & 1) << 1)) << 1;
#pragma unroll
      for (int n = 0; n < TN; ++n) {
        const int col = wn * 96 + n * 16;
        b_offn[tt][n] = lds0 + A_BYTES + ((col >> 6) * 32 + k) * 128 + (((((col & 63) >> 3) + (p >> 1)) ^ f) << 4) + (p & 1) * 8;
      }
    }
  }
  bf16x8 af[2][TM];
  bf16x4 bl[2][TN][2];
  auto rd = [&](int buf, int kt) {
    const unsigned sb = (kt % G1_NS) * G1_ST_BYTES;
    const unsigned aa = a_off + sb;
#pragma unroll
    for (int n = 0; n < TN; ++n) {
      asm volatile("ds_read_b64_tr_b16 %0, %1" : "=v"(bl[buf][n][0]) : "v"(b_offn[0][n] + sb));
      asm volatile("ds_read_b64_tr_b16 %0, %1" : "=v"(bl[buf][n][1]) : "v"(b_offn[1][n] + sb));
    }
#pragma unroll
    for (int m = 0; m < TM; ++m)
      asm volatile("ds_read_b128 %0, %1 offset:%2" : "=v"(af[buf][m]) : "v"(aa), "i"(m * 1024));
  };
  auto mm = [&](int buf) {
#pragma unroll
    for (int m = 0; m < TM; ++m)
#pragma unroll
      for (int n = 0; n < TN; ++n) {
        bf16x8 bb = __builtin_shufflevector(bl[buf][n][0], bl[buf][n][1], 0, 1, 2, 3, 4, 5, 6, 7);
        acc[m][n] = __builtin_amdgcn_mfma_f32_16x16x32_bf16(bb, af[buf][m], acc[m][n], 0, 0, 0);
      }
  };
  if (wid < 4) asm volatile("s_waitcnt vmcnt(12)" ::: "memory"); else asm volatile("s_waitcnt vmcnt(9)" ::: "memory");
  __builtin_amdgcn_s_barrier();
  rd(0, 0);
  auto step = [&](int kt, int buf) {
    if (kt + 3 < NK) { if (wid < 4) asm volatile("s_waitcnt vmcnt(8)" ::: "memory"); else asm volatile("s_waitcnt vmcnt(6)" ::: "memory"); }
    else asm volatile("s_waitcnt vmcnt(0)" ::: "memory");
    asm volatile("s_waitcnt lgkmcnt(0)" ::: "memory");
    __builtin_amdgcn_s_barrier();
    const bool more = kt + 1 < NK;
    const unsigned sb = ((kt + 1) % G1_NS) * G1_ST_BYTES;
    const unsigned aa = a_off + sb;
    const bool ld = kt + G1_NS - 1 < NK;
    const int kn = kt + G1_NS - 1;
    char* nbase = smem + (kn % G1_NS) * G1_ST_BYTES;
    __builtin_amdgcn_sched_barrier(0);
#pragma unroll
    for (int m = 0; m < TM; ++m)
#pragma unroll
      for (int n = 0; n < TN; ++n) {
        bf16x8 bb = __builtin_shufflevector(bl[buf][n][0], bl[buf][n][1], 0, 1, 2, 3, 4, 5, 6, 7);
        acc[m][n] = __builtin_amdgcn_mfma_f32_16x16x32_bf16(bb, af[buf][m], acc[m][n], 0, 0, 0);
        const int i = m * TN + n;
        if (more) {
          if (i < 12) {
            asm volatile("ds_read_b64_tr_b16 %0, %1" : "=v"(bl[buf ^ 1][i >> 1][i & 1]) : "v"(b_offn[i & 1][i >> 1] + sb));
          } else if (i < 16) {
            asm volatile("ds_read_b128 %0, %1 offset:%2" : "=v"(af[buf ^ 1][i - 12]) : "v"(aa), "i"((i - 12) * 1024));
          }
        }
        if (ld) {
          if (i == 3) __builtin_amdgcn_global_load_lds((const unsigned*)(asrc[0] + kn * 32), (LDS_AS unsigned*)(nbase + (wid * 64) * 16), 16, 0, 0);
          if (i == 8) __builtin_amdgcn_global_load_lds((const unsigned*)(asrc[1] + kn * 32), (LDS_AS unsigned*)(nbase + (T + wid * 64) * 16), 16, 0, 0);
          if (i == 13) __builtin_amdgcn_global_load_lds((const unsigned*)(bsrc[0] + (size_t)kn * 32 * bstride), (LDS_AS unsigned*)(nbase + A_BYTES + (wid * 64) * 16), 16, 0, 0);
          if (i == 18 && wid < 4) __builtin_amdgcn_global_load_lds((const unsigned*)(bsrc[1] + (size_t)kn * 32 * bstride), (LDS_AS unsigned*)(nbase + A_BYTES + (T + wid * 64) * 16), 16, 0, 0);
        }
        __builtin_amdgcn_sched_barrier(0);
      }
  };
  for (int kt = 0; kt < NK; kt += 2) { step(kt, 0); step(kt + 1, 1); }
  __builtin_amdgcn_s_barrier();
  constexpr int RS = BN * 2 + 16;
#pragma unroll
  for (int m = 0; m < TM; ++m) {
    const int r = wm * 64 + m * 16 + fr;
    if (isg) {
#pragma unroll
      for (int n = 0; n < TN / 2; ++n) {
        float hv[4];
#pragma unroll
        for (int j = 0; j < 4; ++j) {
          float g = acc[m][n][j], u = acc[m][n + 3][j];
          hv[j] = (g > 0.f ? g : 0.f) * u;
        }
        uint2 o; o.x = pk2(hv[0], hv[1]); o.y = pk2(hv[2], hv[3]);
        *(uint2*)(smem + r * RS + (wn * 48 + n * 16 + fq * 4) * 2) = o;
      }
    } else {
#pragma unroll
      for (int n = 0; n < TN; ++n) {
        float v[4];
#pragma unroll
        for (int j = 0; j < 4; ++j) v[j] = acc[m][n][j];
        uint2 o; o.x = pk2(v[0], v[1]); o.y = pk2(v[2], v[3]);
        *(uint2*)(smem + r * RS + (wn * 96 + n * 16 + fq * 4) * 2) = o;
      }
    }
  }
  __syncthreads();
  if (isg) {
#pragma unroll
    for (int i = 0; i < 6; ++i) {
      int u = i * T + tid, r = u / 12, c = u % 12;
      *(uint4*)(ah + (size_t)(row0 + r) * KAH + D_ + (ct - NQT) * 96 + c * 8) = *(const uint4*)(smem + r * RS + c * 16);
    }
  } else {
#pragma unroll
    for (int i = 0; i < 12; ++i) {
      int u = i * T + tid, r = u / 24, c = u % 24;
      *(uint4*)(qkv + (size_t)(row0 + r) * NQKV + ct * BN + c * 8) = *(const uint4*)(smem + r * RS + c * 16);
    }
  }
}

#define G2_ST (128 * 128 + 64 * 320)
#define G2_NSS 4
__global__ __launch_bounds__(512) void gemm2_kernel(const u16* __restrict__ AH, const u16* __restrict__ Wo,
    const u16* __restrict__ Wd, const u16* __restrict__ X, u16* __restrict__ out, float* slabs, unsigned* cnt) {
  constexpr int T = 512, TM = 2, TN = 5, AB = 16384;
  extern __shared__ __attribute__((aligned(16))) char smem[];
  const int tid = threadIdx.x, lane = tid & 63, wid = tid >> 6;
  const int wm = wid >> 1, wn = wid & 1, fr = lane & 15, fq = lane >> 4;
  const int t = (blockIdx.x & 7) * (gridDim.x >> 3) + (blockIdx.x >> 3);
  const int tile = t >> 2, sp = t & 3;
  const int ct = tile & 3, rt = tile >> 2;
  const int row0 = rt * 128;
  const int k0 = sp == 0 ? 0 : 1 + 9 * sp, NK = sp == 0 ? 10 : 9;
  const u16* asrc[2]; const u16* bsrc[3];
#pragma unroll
  for (int i = 0; i < 2; ++i) {
    int u = i * T + tid, r = u >> 3, slot = u & 7;
    asrc[i] = AH + (size_t)(row0 + r) * KAH + k0 * 64 + ((slot ^ (r & 7)) << 3);
  }
  const u16* Wb = sp == 0 ? Wo : Wd + (size_t)(k0 - 10) * 64 * D_;
#pragma unroll
  for (int i = 0; i < 3; ++i) {
    int u = i * T + tid; if (u >= 1280) u = tid;
    int k = u / 20, slot = u % 20;
    bsrc[i] = Wb + (size_t)k * D_ + ct * 160 + ((slot ^ (((k >> 3) & 1) << 1)) << 3);
  }
  auto gl = [&](int kt, int j) {
    char* base = smem + (kt % G2_NSS) * G2_ST;
    if (j < 2) __builtin_amdgcn_global_load_lds((const unsigned*)(asrc[j] + kt * 64), (LDS_AS unsigned*)(base + (j * T + wid * 64) * 16), 16, 0, 0);
    else if (j < 4 || wid < 4)
      __builtin_amdgcn_global_load_lds((const unsigned*)(bsrc[j - 2] + (size_t)kt * 64 * D_), (LDS_AS unsigned*)(base + AB + ((j - 2) * T + wid * 64) * 16), 16, 0, 0);
  };
#pragma unroll
  for (int s = 0; s < G2_NSS - 1; ++s) { gl(s, 0); gl(s, 1); gl(s, 2); gl(s, 3); gl(s, 4); }
  f32x4 acc[TM][TN];
#pragma unroll
  for (int m = 0; m < TM; ++m)
#pragma unroll
    for (int n = 0; n < TN; ++n) acc[m][n] = (f32x4){0.f, 0.f, 0.f, 0.f};
  const unsigned lds0 = (unsigned)(uintptr_t)(LDS_AS char*)smem;
  unsigned a_off[2], b_off[2][TN];
  {
    const int q = (lane & 15) >> 2, p = lane & 3;
#pragma unroll
    for (int ks = 0; ks < 2; ++ks) a_off[ks] = lds0 + (wm * 32 + fr) * 128 + (((ks * 4 + fq) ^ (fr & 7)) << 4);
#pragma unroll
    for (int tt = 0; tt < 2; ++tt) {
      const int k = 8 * fq + 4 * tt + q;
#pragma unroll
      for (int n = 0; n < TN; ++n)
        b_off[tt][n] = lds0 + AB + k * 320 + ((((wn * 80 + n * 16) >> 3) + (p >> 1)) ^ (((k >> 3) & 1) << 1)) * 16 + (p & 1) * 8;
    }
  }
  bf16x8 af[2][2][TM];
  bf16x4 bl[2][2][TN][2];
  auto rd1 = [&](int buf, unsigned sb, int i) {
    if (i < 20) { const int ks = i / 10, n = (i % 10) >> 1, tt = i & 1;
      asm volatile("ds_read_b64_tr_b16 %0, %1 offset:%2" : "=v"(bl[buf][ks][n][tt]) : "v"(b_off[tt][n] + sb), "i"(ks * 32 * 320)); }
    else { const int ks = (i - 20) >> 1, m = (i - 20) & 1;
      asm volatile("ds_read_b128 %0, %1 offset:%2" : "=v"(af[buf][ks][m]) : "v"(a_off[ks] + sb), "i"(m * 2048)); }
  };
  if (wid < 4) asm volatile("s_waitcnt vmcnt(10)" ::: "memory"); else asm volatile("s_waitcnt vmcnt(8)" ::: "memory");
  __builtin_amdgcn_s_barrier();
#pragma unroll
  for (int i = 0; i < 24; ++i) rd1(0, 0, i);
  auto step = [&](int kt, int buf) {
    if (kt + 2 < NK) { if (wid < 4) asm volatile("s_waitcnt vmcnt(5)" ::: "memory"); else asm volatile("s_waitcnt vmcnt(4)" ::: "memory"); }
    else asm volatile("s_waitcnt vmcnt(0)" ::: "memory");
    asm volatile("s_waitcnt lgkmcnt(0)" ::: "memory");
    __builtin_amdgcn_s_barrier();
    const bool more = kt + 1 < NK, ld = kt + G2_NSS - 1 < NK;
    const unsigned sb = ((kt + 1) % G2_NSS) * G2_ST;
    __builtin_amdgcn_sched_barrier(0);
#pragma unroll
    for (int i = 0; i < 20; ++i) {
      const int ks = i / 10, m = (i % 10) / 5, n = i % 5;
      bf16x8 bb = __builtin_shufflevector(bl[buf][ks][n][0], bl[buf][ks][n][1], 0, 1, 2, 3, 4, 5, 6, 7);
      acc[m][n] = __builtin_amdgcn_mfma_f32_16x16x32_bf16(bb, af[buf][ks][m], acc[m][n], 0, 0, 0);
      if (more) { rd1(buf ^ 1, sb, i); if (i < 4) rd1(buf ^ 1, sb, 20 + i); }
      if (ld && (i % 4) == 1) gl(kt + G2_NSS - 1, i >> 2);
      __builtin_amdgcn_sched_barrier(0);
    }
  };
  int kt = 0;
  for (; kt + 1 < NK; kt += 2) { step(kt, 0); step(kt + 1, 1); }
  if (kt < NK) step(kt, 0);

  float* slab = slabs + (size_t)(tile * 4 + sp) * (128 * 160);
#pragma unroll
  for (int m = 0; m < TM; ++m)
#pragma unroll
    for (int n = 0; n < TN; ++n)
      *(f32x4*)(slab + (wm * 32 + m * 16 + fr) * 160 + wn * 80 + n * 16 + fq * 4) = acc[m][n];
  asm volatile("s_waitcnt vmcnt(0)" ::: "memory");
  __syncthreads();
  if (tid == 0) {
    __builtin_amdgcn_fence(__ATOMIC_RELEASE, "agent");
    asm volatile("s_waitcnt vmcnt(0)" ::: "memory");
    __hip_atomic_fetch_add(&cnt[tile], 1u, __ATOMIC_RELAXED, __HIP_MEMORY_SCOPE_AGENT);
    unsigned spins = 0;
    while (__hip_atomic_load(&cnt[tile], __ATOMIC_RELAXED, __HIP_MEMORY_SCOPE_AGENT) < 4u && spins < (1u << 26)) { __builtin_amdgcn_s_sleep(1); ++spins; }
    __builtin_amdgcn_fence(__ATOMIC_ACQUIRE, "agent");
    asm volatile("s_waitcnt vmcnt(0)" ::: "memory");
  }
  __syncthreads();
  const float* s0p = slabs + (size_t)(tile * 4) * (128 * 160);
  for (int u = tid; u < 640; u += T) {
    const int r = sp * 32 + u / 20, c = (u % 20) * 8;
    f32x4 a0 = {0.f, 0.f, 0.f, 0.f}, a1 = {0.f, 0.f, 0.f, 0.f};
#pragma unroll
    for (int s = 0; s < 4; ++s) {
      const float* p = s0p + (size_t)s * (128 * 160) + r * 160 + c;
      a0 += *(const f32x4*)p; a1 += *(const f32x4*)(p + 4);
    }
    size_t go = (size_t)(row0 + r) * D_ + ct * 160 + c;
    uint4 xv = *(const uint4*)(X + go);
    uint4 o;
    o.x = pk2(a0[0] + __uint_as_float(xv.x << 16), a0[1] + __uint_as_float(xv.x & 0xffff0000u));
    o.y = pk2(a0[2] + __uint_as_float(xv.y << 16), a0[3] + __uint_as_float(xv.y & 0xffff0000u));
    o.z = pk2(a1[0] + __uint_as_float(xv.z << 16), a1[1] + __uint_as_float(xv.z & 0xffff0000u));
    o.w = pk2(a1[2] + __uint_as_float(xv.w << 16), a1[3] + __uint_as_float(xv.w & 0xffff0000u));
    *(uint4*)(out + go) = o;
  }
}

__global__ __launch_bounds__(256) void attn_kernel(const u16* __restrict__ qkv, u16* __restrict__ ah) {
  int idx = blockIdx.x * 256 + threadIdx.x;
  int h = idx & 31, s = idx >> 5;
  float q[20];
  {
    const uint2* p = (const uint2*)(qkv + (size_t)s * NQKV + h * 20);
#pragma unroll
    for (int i = 0; i < 5; ++i) {
      uint2 t = p[i];
      q[i * 4 + 0] = __uint_as_float(t.x << 16); q[i * 4 + 1] = __uint_as_float(t.x & 0xffff0000u);
      q[i * 4 + 2] = __uint_as_float(t.y << 16); q[i * 4 + 3] = __uint_as_float(t.y & 0xffff0000u);
    }
  }
  float lg[8];
  const float scale = 0.22360679774997896f;
#pragma unroll
  for (int w = 0; w < 8; ++w) {
    int ks = s - 2 * w;
    int kc = ks >= 0 ? ks : 0;
    const uint2* p = (const uint2*)(qkv + (size_t)kc * NQKV + D_ + h * 20);
    float a = 0.f;
#pragma unroll
    for (int i = 0; i < 5; ++i) {
      uint2 t = p[i];
      a += q[i * 4 + 0] * __uint_as_float(t.x << 16) + q[i * 4 + 1] * __uint_as_float(t.x & 0xffff0000u) +
           q[i * 4 + 2] * __uint_as_float(t.y << 16) + q[i * 4 + 3] * __uint_as_float(t.y & 0xffff0000u);
    }
    lg[w] = ks >= 0 ? a * scale : -1e9f;
  }
  float mx = lg[0];
#pragma unroll
  for (int w = 1; w < 8; ++w) mx = fmaxf(mx, lg[w]);
  float sum = 0.f;
#pragma unroll
  for (int w = 0; w < 8; ++w) { lg[w] = __expf(lg[w] - mx); sum += lg[w]; }
  float inv = 1.0f / sum;
  float o[20];
#pragma unroll
  for (int i = 0; i < 20; ++i) o[i] = 0.f;
#pragma unroll
  for (int w = 0; w < 8; ++w) {
    int ks = s - 2 * w;
    int kc = ks >= 0 ? ks : 0;
    const uint2* p = (const uint2*)(qkv + (size_t)kc * NQKV + 2 * D_ + h * 20);
    float pw = lg[w] * inv;
#pragma unroll
    for (int i = 0; i < 5; ++i) {
      uint2 t = p[i];
      o[i * 4 + 0] += pw * __uint_as_float(t.x << 16); o[i * 4 + 1] += pw * __uint_as_float(t.x & 0xffff0000u);
      o[i * 4 + 2] += pw * __uint_as_float(t.y << 16); o[i * 4 + 3] += pw * __uint_as_float(t.y & 0xffff0000u);
    }
  }
  uint2* op = (uint2*)(ah + (size_t)s * KAH + h * 20);
#pragma unroll
  for (int i = 0; i < 5; ++i) {
    uint2 t;
    t.x = (unsigned)f2bf(o[i * 4 + 0]) | ((unsigned)f2bf(o[i * 4 + 1]) << 16);
    t.y = (unsigned)f2bf(o[i * 4 + 2]) | ((unsigned)f2bf(o[i * 4 + 3]) << 16);
    op[i] = t;
  }
}

extern "C" void kernel_launch(void* const* d_in, const int* in_sizes, int n_in,
                              void* d_out, int out_size, void* d_ws, size_t ws_size,
                              hipStream_t stream) {
  const u16* x = (const u16*)d_in[0];
  const u16* wqkv = (const u16*)d_in[1];
  const u16* wo = (const u16*)d_in[2];
  const u16* wg = (const u16*)d_in[4];
  const u16* wu = (const u16*)d_in[5];
  const u16* wd = (const u16*)d_in[6];
  u16* out = (u16*)d_out;
  u16* xn = (u16*)d_ws;
  u16* qkv = xn + S_ * D_;
  u16* ah = qkv + S_ * NQKV;
  float* slabs = (float*)(ah + S_ * KAH);
  unsigned* cnt = (unsigned*)(slabs + 64 * 4 * 128 * 160);

  {
    int lds = G1_NS * G1_ST_BYTES + 256 * 8;
    hipFuncSetAttribute((const void*)gemm1_kernel, hipFuncAttributeMaxDynamicSharedMemorySize, lds);
    ln_kernel<<<S_ / 4, 256, 0, stream>>>(x, xn, cnt);
    gemm1_kernel<<<224, 512, lds, stream>>>(xn, wqkv, wg, wu, qkv, ah);
  }
  attn_kernel<<<S_ * 32 / 256, 256, 0, stream>>>(qkv, ah);
  {
    int lds = G2_NSS * G2_ST;
    hipFuncSetAttribute((const void*)gemm2_kernel, hipFuncAttributeMaxDynamicSharedMemorySize, lds);
    gemm2_kernel<<<256, 512, lds, stream>>>(ah, wo, wd, x, out, slabs, cnt);
  }
}
```

```cpp
#define G1BM 256
#define G1BN 192
#define G1NS 2
#define G1WM 4
#define G1WN 2
#define G2BM 64
#define G2BN 64
#define G2NS 4
#define G2WM 2
#define G2WN 2
#include <hip/hip_runtime.h>
#include <hip/hip_bf16.h>
#include <stdint.h>

typedef __attribute__((ext_vector_type(8))) short bf16x8;
typedef __attribute__((ext_vector_type(4))) short bf16x4;
typedef __attribute__((ext_vector_type(4))) float f32x4;
typedef unsigned short u16;
#define LDS_AS __attribute__((address_space(3)))

#define S_ 2048
#define D_ 640
#define F_ 1728
#define NQKV 1920
#define KAH 2368

__device__ __forceinline__ float bf2f(u16 v) { return __uint_as_float(((unsigned)v) << 16); }
__device__ __forceinline__ u16 f2bf(float f) {
  unsigned u = __float_as_uint(f);
  u += 0x7FFF + ((u >> 16) & 1);
  return (u16)(u >> 16);
}

__device__ __forceinline__ unsigned pk2(float a, float b) {
  typedef __attribute__((ext_vector_type(2))) __bf16 bf2;
  bf2 v; v[0] = (__bf16)a; v[1] = (__bf16)b;
  return __builtin_bit_cast(unsigned, v);
}
__global__ __launch_bounds__(256) void ln_kernel(const u16* __restrict__ x, u16* __restrict__ xn) {
  int row = blockIdx.x * 4 + (threadIdx.x >> 6);
  int lane = threadIdx.x & 63;
  const u16* xr = x + (size_t)row * D_;
  float v[10];
  float s = 0.f;
#pragma unroll
  for (int i = 0; i < 10; ++i) { v[i] = bf2f(xr[lane + i * 64]); s += v[i]; }
#pragma unroll
  for (int o = 32; o > 0; o >>= 1) s += __shfl_xor(s, o);
  float m = s * (1.0f / D_);
  float q = 0.f;
#pragma unroll
  for (int i = 0; i < 10; ++i) { float d = v[i] - m; q += d * d; }
#pragma unroll
  for (int o = 32; o > 0; o >>= 1) q += __shfl_xor(q, o);
  float rs = rsqrtf(q * (1.0f / D_) + 1e-5f);
#pragma unroll
  for (int i = 0; i < 10; ++i) xn[(size_t)row * D_ + lane + i * 64] = f2bf((v[i] - m) * rs);
}

template <int MODE, int BM, int BN, int WM, int WN, int NSTAGE>
__global__ __launch_bounds__(WM* WN * 64) void gemm_kernel(
    const u16* __restrict__ A, int lda, int nk,
    const u16* __restrict__ W0, const u16* __restrict__ W1, const u16* __restrict__ W2,
    u16* __restrict__ out0, u16* __restrict__ out1, const u16* __restrict__ xres) {
  constexpr int T = WM * WN * 64;
  constexpr int TM = BM / WM / 16, TN = BN / WN / 16;
  constexpr int NB = BN / 16;
  constexpr int A_BYTES = BM * 128, B_BYTES = 64 * BN * 2, ST_BYTES = A_BYTES + B_BYTES;
  constexpr int AU = A_BYTES / 16 / T, BU = B_BYTES / 16 / T;
  static_assert(AU * 16 * T == A_BYTES && BU * 16 * T == B_BYTES, "units");
  extern __shared__ __attribute__((aligned(16))) char smem[];

  const int tid = threadIdx.x, lane = tid & 63, wid = tid >> 6;
  const int wm = wid / WN, wn = wid % WN;
  const int fr = lane & 15, fq = lane >> 4;
  int ct, rt;
  constexpr int NQT = NQKV / BN, NCT = NQT + 2 * F_ / BN;
  {
    int t = (blockIdx.x & 7) * (gridDim.x >> 3) + (blockIdx.x >> 3);
    if (MODE == 1) { ct = t / (S_ / BM); rt = t % (S_ / BM); }
    else { ct = t % (D_ / BN); rt = t / (D_ / BN); }
  }
  const int row0 = rt * BM;

  const u16* asrc[AU];
  const u16* bsrc[BU];
  int bstride;
#pragma unroll
  for (int i = 0; i < AU; ++i) {
    int u = i * T + tid, r = u >> 3, slot = u & 7;
    asrc[i] = A + (size_t)(row0 + r) * lda + ((slot ^ (r & 7)) << 3);
  }
#pragma unroll
  for (int i = 0; i < BU; ++i) {
    int u = i * T + tid, s = u >> 4, rp = (u & 15) >> 1, half = u & 1;
    int kb = s / NB, nb = s % NB;
    int k = kb * 8 + (rp ^ ((kb & 1) << 2));
    int c = nb * 16 + half * 8;
    if (MODE == 1) {
      if (ct < NQT) { bsrc[i] = W0 + (size_t)k * NQKV + ct * BN + c; bstride = NQKV; }
      else {
        int j = ct - NQT;
        constexpr int WC = BN / WN, HC = WC / 2;
        int cwn = c / WC, cw = c % WC;
        const u16* w = (cw >= HC) ? W2 : W1;
        bsrc[i] = w + (size_t)k * F_ + j * (BN / 2) + cwn * HC + (cw % HC);
        bstride = F_;
      }
    } else {
      bsrc[i] = W0 + (size_t)k * D_ + ct * BN + c;
      bstride = D_;
    }
  }

  auto stage = [&](int kt) {
    char* base = smem + (kt % NSTAGE) * ST_BYTES;
#pragma unroll
    for (int i = 0; i < AU; ++i)
      __builtin_amdgcn_global_load_lds((const unsigned*)(asrc[i] + kt * 64),
                                       (LDS_AS unsigned*)(base + (i * T + wid * 64) * 16), 16, 0, 0);
#pragma unroll
    for (int i = 0; i < BU; ++i) {
      const u16* p;
      if (MODE == 1) p = bsrc[i] + (size_t)kt * 64 * bstride;
      else {
        if (kt < 10) p = bsrc[i] + (size_t)kt * 64 * D_;
        else p = (bsrc[i] - W0 + W1) + (size_t)(kt - 10) * 64 * D_;
      }
      __builtin_amdgcn_global_load_lds((const unsigned*)p,
                                       (LDS_AS unsigned*)(base + A_BYTES + (i * T + wid * 64) * 16), 16, 0, 0);
    }
  };

  f32x4 acc[TM][TN];
#pragma unroll
  for (int m = 0; m < TM; ++m)
#pragma unroll
    for (int n = 0; n < TN; ++n) acc[m][n] = (f32x4){0.f, 0.f, 0.f, 0.f};

#pragma unroll
  for (int s = 0; s < NSTAGE - 1; ++s) stage(s);

  const unsigned lds0 = (unsigned)(uintptr_t)(LDS_AS char*)smem;
  unsigned a_off[2], b_off[2];
  {
    const int q = (lane & 15) >> 2, p = lane & 3;
#pragma unroll
    for (int ks = 0; ks < 2; ++ks)
      a_off[ks] = lds0 + (wm * (BM / WM) + fr) * 128 + (((ks * 4 + fq) ^ (fr & 7)) << 4);
#pragma unroll
    for (int t = 0; t < 2; ++t)
      b_off[t] = lds0 + A_BYTES + (fq * NB + wn * TN) * 256 + (4 * (t ^ (fq & 1)) + q) * 32 + p * 8;
  }
  for (int kt = 0; kt < nk; ++kt) {
    if (kt + NSTAGE - 2 < nk) {
      asm volatile("s_waitcnt vmcnt(%0)" ::"i"((NSTAGE - 2) * (AU + BU)) : "memory");
    } else {
      asm volatile("s_waitcnt vmcnt(0)" ::: "memory");
    }
    __builtin_amdgcn_s_barrier();
    const unsigned sb = (kt % NSTAGE) * ST_BYTES;
    bf16x8 af[2][TM];
    bf16x4 bl[2][TN][2];
#pragma unroll
    for (int ks = 0; ks < 2; ++ks) {
      unsigned aa = a_off[ks] + sb, b0 = b_off[0] + sb, b1 = b_off[1] + sb;
#pragma unroll
      for (int m = 0; m < TM; ++m)
        asm volatile("ds_read_b128 %0, %1 offset:%2" : "=v"(af[ks][m]) : "v"(aa), "i"(m * 2048));
#pragma unroll
      for (int n = 0; n < TN; ++n) {
        asm volatile("ds_read_b64_tr_b16 %0, %1 offset:%2" : "=v"(bl[ks][n][0]) : "v"(b0), "i"(ks * 4 * NB * 256 + n * 256));
        asm volatile("ds_read_b64_tr_b16 %0, %1 offset:%2" : "=v"(bl[ks][n][1]) : "v"(b1), "i"(ks * 4 * NB * 256 + n * 256));
      }
    }
    if (kt + NSTAGE - 1 < nk) stage(kt + NSTAGE - 1);
#pragma unroll
    for (int ks = 0; ks < 2; ++ks) {
      if (ks == 0) asm volatile("s_waitcnt lgkmcnt(%0)" ::"i"((TM + 2 * TN) > 15 ? 15 : (TM + 2 * TN)) : "memory");
      else asm volatile("s_waitcnt lgkmcnt(0)" ::: "memory");
      __builtin_amdgcn_sched_barrier(0);
#pragma unroll
      for (int m = 0; m < TM; ++m)
#pragma unroll
        for (int n = 0; n < TN; ++n) {
          bf16x8 bb = __builtin_shufflevector(bl[ks][n][0], bl[ks][n][1], 0, 1, 2, 3, 4, 5, 6, 7);
          acc[m][n] = __builtin_amdgcn_mfma_f32_16x16x32_bf16(bb, af[ks][m], acc[m][n], 0, 0, 0);
        }
      __builtin_amdgcn_sched_barrier(0);
    }
  }
  __builtin_amdgcn_s_barrier();
  if (MODE == 1) {
    const bool isg = ct >= NQT;
    constexpr int RS = BN * 2 + 16;
    if (isg) {
#pragma unroll
      for (int m = 0; m < TM; ++m)
#pragma unroll
        for (int n = 0; n < TN / 2; ++n) {
          float hv[4];
#pragma unroll
          for (int j = 0; j < 4; ++j) { float g = acc[m][n][j]; hv[j] = (g > 0.f ? g : 0.f) * acc[m][n + TN / 2][j]; }
          uint2 o; o.x = pk2(hv[0], hv[1]); o.y = pk2(hv[2], hv[3]);
          int r = wm * (BM / WM) + m * 16 + fr;
          int c = wn * (BN / WN / 2) + n * 16 + fq * 4;
          *(uint2*)(smem + r * RS + c * 2) = o;
        }
    } else {
#pragma unroll
      for (int m = 0; m < TM; ++m)
#pragma unroll
        for (int n = 0; n < TN; ++n) {
          uint2 o; o.x = pk2(acc[m][n][0], acc[m][n][1]); o.y = pk2(acc[m][n][2], acc[m][n][3]);
          int r = wm * (BM / WM) + m * 16 + fr;
          int c = wn * (BN / WN) + n * 16 + fq * 4;
          *(uint2*)(smem + r * RS + c * 2) = o;
        }
    }
    __syncthreads();
    if (isg) {
      constexpr int CPR = BN / 16;
#pragma unroll
      for (int i = 0; i < BM * CPR / T; ++i) {
        int u = i * T + tid, r = u / CPR, c = u % CPR;
        *(uint4*)(out1 + (size_t)(row0 + r) * KAH + D_ + (ct - NQT) * (BN / 2) + c * 8) = *(const uint4*)(smem + r * RS + c * 16);
      }
    } else {
      constexpr int CPR = BN / 8;
#pragma unroll
      for (int i = 0; i < BM * CPR / T; ++i) {
        int u = i * T + tid, r = u / CPR, c = u % CPR;
        *(uint4*)(out0 + (size_t)(row0 + r) * NQKV + ct * BN + c * 8) = *(const uint4*)(smem + r * RS + c * 16);
      }
    }
  } else {
    constexpr int RS = BN * 4 + 16;
#pragma unroll
    for (int m = 0; m < TM; ++m)
#pragma unroll
      for (int n = 0; n < TN; ++n) {
        int r = wm * (BM / WM) + m * 16 + fr;
        int c = wn * (BN / WN) + n * 16 + fq * 4;
        *(f32x4*)(smem + r * RS + c * 4) = acc[m][n];
      }
    __syncthreads();
    constexpr int CPR = BN / 8;
#pragma unroll
    for (int i = 0; i < BM * CPR / T; ++i) {
      int u = i * T + tid, r = u / CPR, c = u % CPR;
      const f32x4 s0 = *(const f32x4*)(smem + r * RS + c * 32), s1 = *(const f32x4*)(smem + r * RS + c * 32 + 16);
      size_t go = (size_t)(row0 + r) * D_ + ct * BN + c * 8;
      uint4 xv = *(const uint4*)(xres + go);
      uint4 o;
      o.x = pk2(s0[0] + __uint_as_float(xv.x << 16), s0[1] + __uint_as_float(xv.x & 0xffff0000u));
      o.y = pk2(s0[2] + __uint_as_float(xv.y << 16), s0[3] + __uint_as_float(xv.y & 0xffff0000u));
      o.z = pk2(s1[0] + __uint_as_float(xv.z << 16), s1[1] + __uint_as_float(xv.z & 0xffff0000u));
      o.w = pk2(s1[2] + __uint_as_float(xv.w << 16), s1[3] + __uint_as_float(xv.w & 0xffff0000u));
      *(uint4*)(out0 + go) = o;
    }
  }
}

__global__ __launch_bounds__(256) void attn_kernel(const u16* __restrict__ qkv, u16* __restrict__ ah) {
  int idx = blockIdx.x * 256 + threadIdx.x;
  int h = idx & 31, s = idx >> 5;
  float q[20];
  {
    const uint2* p = (const uint2*)(qkv + (size_t)s * NQKV + h * 20);
#pragma unroll
    for (int i = 0; i < 5; ++i) {
      uint2 t = p[i];
      q[i * 4 + 0] = __uint_as_float(t.x << 16); q[i * 4 + 1] = __uint_as_float(t.x & 0xffff0000u);
      q[i * 4 + 2] = __uint_as_float(t.y << 16); q[i * 4 + 3] = __uint_as_float(t.y & 0xffff0000u);
    }
  }
  float lg[8];
  const float scale = 0.22360679774997896f;
#pragma unroll
  for (int w = 0; w < 8; ++w) {
    int ks = s - 2 * w;
    int kc = ks >= 0 ? ks : 0;
    const uint2* p = (const uint2*)(qkv + (size_t)kc * NQKV + D_ + h * 20);
    float a = 0.f;
#pragma unroll
    for (int i = 0; i < 5; ++i) {
      uint2 t = p[i];
      a += q[i * 4 + 0] * __uint_as_float(t.x << 16) + q[i * 4 + 1] * __uint_as_float(t.x & 0xffff0000u) +
           q[i * 4 + 2] * __uint_as_float(t.y << 16) + q[i * 4 + 3] * __uint_as_float(t.y & 0xffff0000u);
    }
    lg[w] = ks >= 0 ? a * scale : -1e9f;
  }
  float mx = lg[0];
#pragma unroll
  for (int w = 1; w < 8; ++w) mx = fmaxf(mx, lg[w]);
  float sum = 0.f;
#pragma unroll
  for (int w = 0; w < 8; ++w) { lg[w] = __expf(lg[w] - mx); sum += lg[w]; }
  float inv = 1.0f / sum;
  float o[20];
#pragma unroll
  for (int i = 0; i < 20; ++i) o[i] = 0.f;
#pragma unroll
  for (int w = 0; w < 8; ++w) {
    int ks = s - 2 * w;
    int kc = ks >= 0 ? ks : 0;
    const uint2* p = (const uint2*)(qkv + (size_t)kc * NQKV + 2 * D_ + h * 20);
    float pw = lg[w] * inv;
#pragma unroll
    for (int i = 0; i < 5; ++i) {
      uint2 t = p[i];
      o[i * 4 + 0] += pw * __uint_as_float(t.x << 16); o[i * 4 + 1] += pw * __uint_as_float(t.x & 0xffff0000u);
      o[i * 4 + 2] += pw * __uint_as_float(t.y << 16); o[i * 4 + 3] += pw * __uint_as_float(t.y & 0xffff0000u);
    }
  }
  uint2* op = (uint2*)(ah + (size_t)s * KAH + h * 20);
#pragma unroll
  for (int i = 0; i < 5; ++i) {
    uint2 t;
    t.x = (unsigned)f2bf(o[i * 4 + 0]) | ((unsigned)f2bf(o[i * 4 + 1]) << 16);
    t.y = (unsigned)f2bf(o[i * 4 + 2]) | ((unsigned)f2bf(o[i * 4 + 3]) << 16);
    op[i] = t;
  }
}

extern "C" void kernel_launch(void* const* d_in, const int* in_sizes, int n_in,
                              void* d_out, int out_size, void* d_ws, size_t ws_size,
                              hipStream_t stream) {
  const u16* x = (const u16*)d_in[0];
  const u16* wqkv = (const u16*)d_in[1];
  const u16* wo = (const u16*)d_in[2];
  const u16* wg = (const u16*)d_in[4];
  const u16* wu = (const u16*)d_in[5];
  const u16* wd = (const u16*)d_in[6];
  u16* out = (u16*)d_out;
  u16* xn = (u16*)d_ws;
  u16* qkv = xn + S_ * D_;
  u16* ah = qkv + S_ * NQKV;

  ln_kernel<<<S_ / 4, 256, 0, stream>>>(x, xn);
  {
    constexpr int BM = G1BM, BN = G1BN, NS = G1NS;
    auto k = gemm_kernel<1, BM, BN, G1WM, G1WN, NS>;
    int lds = NS * (BM * 128 + 64 * BN * 2);
    hipFuncSetAttribute((const void*)k, hipFuncAttributeMaxDynamicSharedMemorySize, lds);
    k<<<(S_ / BM) * (5376 / BN), G1WM * G1WN * 64, lds, stream>>>(xn, D_, 10, wqkv, wg, wu, qkv, ah, nullptr);
  }
  attn_kernel<<<S_ * 32 / 256, 256, 0, stream>>>(qkv, ah);
  {
    constexpr int BM = G2BM, BN = G2BN, NS = G2NS;
    auto k = gemm_kernel<2, BM, BN, G2WM, G2WN, NS>;
    int lds = NS * (BM * 128 + 64 * BN * 2);
    hipFuncSetAttribute((const void*)k, hipFuncAttributeMaxDynamicSharedMemorySize, lds);
    k<<<(S_ / BM) * (D_ / BN), G2WM * G2WN * 64, lds, stream>>>(ah, KAH, 37, wo, wd, nullptr, out, nullptr, x);
  }
}
```
